```python
import torch

def _softmax(x, axis=-1):
    return torch.softmax(x, dim=axis)
def _gelu(x):
    return torch.nn.functional.gelu(x, approximate="tanh")
def _silu(x):
    return torch.nn.functional.silu(x)
def _sigmoid(x):
    return torch.sigmoid(x)
def _layernorm(x, axis=-1, eps=1e-5):
    m = x.mean(dim=axis, keepdim=True)
    v = x.var(dim=axis, keepdim=True, unbiased=False)
    return (x - m) / torch.sqrt(v + eps)
def _scatter_add(tgt, idx, src, axis=0):
    return tgt.clone().index_add_(axis, idx, src)
def _segment_sum(src, seg, n):
    out = torch.zeros((n,) + src.shape[1:], dtype=src.dtype)
    return out.index_add_(0, seg, src)
def _pad(x, pw):
    flat = tuple(v for pair in reversed(pw) for v in pair)
    return torch.nn.functional.pad(x, flat)

kernel_name = 'standalone__layernorm_linear__sampling'

def setup_inputs():

    _w = (lambda a: (lambda t: t.to(torch.bfloat16) if t.is_floating_point() else t)(torch.as_tensor(a)))
    B, S, D = 1024, 8, 576
    return {
        "x": _w((lambda *s: torch.randn(*s, dtype=torch.bfloat16) * 0.05)(B, S, D)),
        "g": _w((lambda *s: torch.randn(*s, dtype=torch.bfloat16) * 0.05)(D) + 1.0),
        "b": _w((lambda *s: torch.randn(*s, dtype=torch.bfloat16) * 0.4)(D)),
        "w": _w((lambda *s: torch.randn(*s, dtype=torch.bfloat16) * 0.05)(D, D)),
    }

def reference(x, g, b, w):
    return (_layernorm(x, axis=-1) * g + b) @ w
```

```cpp
#include <hip/hip_runtime.h>
#include <hip/hip_bf16.h>
#include <stdint.h>

typedef __attribute__((ext_vector_type(8))) short bf16x8;
typedef __attribute__((ext_vector_type(4))) float f32x4;
typedef __attribute__((ext_vector_type(4))) unsigned int u32x4;

constexpr int D = 576;

__device__ __forceinline__ float bflo(unsigned v) { return __uint_as_float(v << 16); }
__device__ __forceinline__ float bfhi(unsigned v) { return __uint_as_float(v & 0xffff0000u); }
__device__ __forceinline__ unsigned pack2(float a, float b) {
    __hip_bfloat162 r = __float22bfloat162_rn(make_float2(a, b));
    return *reinterpret_cast<unsigned*>(&r);
}

__global__ __launch_bounds__(256) void ln_lin_simple(const unsigned short* __restrict__ x,
                                                     const unsigned short* __restrict__ g,
                                                     const unsigned short* __restrict__ b,
                                                     const unsigned short* __restrict__ w,
                                                     unsigned short* __restrict__ out) {
    const int lane = threadIdx.x & 63, wid = threadIdx.x >> 6;
    const int r = lane & 15, q = lane >> 4;
    const int nt = blockIdx.x & 3, mt = blockIdx.x >> 2;
    const int row = mt * 64 + wid * 16 + r;
    u32x4 xa[18];
    const u32x4* xp = reinterpret_cast<const u32x4*>(x + (size_t)row * D + q * 8);
#pragma unroll
    for (int s = 0; s < 18; ++s) xa[s] = xp[s * 4];
    float sum = 0.f;
#pragma unroll
    for (int s = 0; s < 18; ++s)
#pragma unroll
        for (int j = 0; j < 4; ++j) sum += bflo(xa[s][j]) + bfhi(xa[s][j]);
    sum += __shfl_xor(sum, 16);
    sum += __shfl_xor(sum, 32);
    const float mean = sum * (1.f / D);
    float sq = 0.f;
#pragma unroll
    for (int s = 0; s < 18; ++s)
#pragma unroll
        for (int j = 0; j < 4; ++j) {
            float a = bflo(xa[s][j]) - mean, c = bfhi(xa[s][j]) - mean;
            sq += a * a + c * c;
        }
    sq += __shfl_xor(sq, 16);
    sq += __shfl_xor(sq, 32);
    const float rstd = 1.f / sqrtf(sq * (1.f / D) + 1e-5f);
    const u32x4* gp = reinterpret_cast<const u32x4*>(g + q * 8);
    const u32x4* bp = reinterpret_cast<const u32x4*>(b + q * 8);
    f32x4 acc[9];
#pragma unroll
    for (int n = 0; n < 9; ++n) acc[n] = f32x4{0.f, 0.f, 0.f, 0.f};
#pragma unroll
    for (int s = 0; s < 18; ++s) {
        u32x4 gv = gp[s * 4], bv = bp[s * 4], y;
#pragma unroll
        for (int j = 0; j < 4; ++j) {
            float a = (bflo(xa[s][j]) - mean) * rstd * bflo(gv[j]) + bflo(bv[j]);
            float c = (bfhi(xa[s][j]) - mean) * rstd * bfhi(gv[j]) + bfhi(bv[j]);
            y[j] = pack2(a, c);
        }
        bf16x8 yf = __builtin_bit_cast(bf16x8, y);
#pragma unroll
        for (int n = 0; n < 9; ++n) {
            bf16x8 wf;
            const unsigned short* wp = w + (size_t)(s * 32 + q * 8) * D + nt * 144 + n * 16 + r;
#pragma unroll
            for (int j = 0; j < 8; ++j) wf[j] = (short)wp[j * D];
            acc[n] = __builtin_amdgcn_mfma_f32_16x16x32_bf16(wf, yf, acc[n], 0, 0, 0);
        }
    }
#pragma unroll
    for (int n = 0; n < 9; ++n) {
        uint2 o = make_uint2(pack2(acc[n][0], acc[n][1]), pack2(acc[n][2], acc[n][3]));
        *reinterpret_cast<uint2*>(out + (size_t)row * D + nt * 144 + n * 16 + q * 4) = o;
    }
}

extern "C" void kernel_launch(void* const* d_in, const int* in_sizes, int n_in, void* d_out, int out_size,
                              void* d_ws, size_t ws_size, hipStream_t stream) {
    const int M = in_sizes[0] / D;
    ln_lin_simple<<<(M / 64) * 4, 256, 0, stream>>>((const unsigned short*)d_in[0], (const unsigned short*)d_in[1],
                                                    (const unsigned short*)d_in[2], (const unsigned short*)d_in[3],
                                                    (unsigned short*)d_out);
}
```

```cpp
#include <hip/hip_runtime.h>
#include <stdint.h>

__global__ __launch_bounds__(512) void ln_linear_kernel(const void* x, const void* g, const void* b, const void* w, void* out) {
    __shared__ __attribute__((aligned(16))) char lds[163840];
    asm volatile(
        "s_load_dwordx2 s[8:9], %0, 0\n"
        "s_load_dwordx2 s[12:13], %0, 8\n"
        "s_load_dwordx2 s[16:17], %0, 16\n"
        "s_load_dwordx2 s[20:21], %0, 24\n"
        "s_load_dwordx2 s[24:25], %0, 32\n"
        "v_readfirstlane_b32 s28, v0\n"
        "s_getpc_b64 s[90:91]\n"
        "L%=_pc:\n"
        "s_lshr_b32 s29, %1, 3\n"
        "s_cmp_ge_u32 s28, 64\n"
        "s_cbranch_scc1 L%=_novpf\n"
        "s_mul_i32 s30, s29, 43\n"
        "s_lshr_b32 s30, s30, 8\n"
        "s_mul_i32 s30, s30, 6\n"
        "s_sub_u32 s29, s29, s30\n"
        "s_lshl_b32 s29, s29, 12\n"
        "s_and_b32 s90, s90, -128\n"
        "v_lshrrev_b32 v194, 5, v0\n"
        "v_mul_u32_u24 v194, s29, v194\n"
        "v_lshl_add_u32 v194, v0, 7, v194\n"
        "v_min_u32 v194, L%=_end-L%=_pc, v194\n"
        "global_load_dword v195, v194, s[90:91]\n"
        "L%=_novpf:\n"
        "s_mov_b32 s10, 9437184\n"
        "s_mov_b32 s11, 0x20000\n"
        "s_movk_i32 s14, 1152\n"
        "s_mov_b32 s15, 0x20000\n"
        "s_lshr_b32 s28, s28, 6\n"
        "s_lshr_b32 s47, s28, 2\n"
        "s_and_b32 s46, s28, 3\n"
        "v_lshrrev_b32 v1, 4, v0\n"
        "v_and_b32 v2, 15, v0\n"
        "v_mul_u32_u24 v3, 1152, v1\n"
        "v_lshl_add_u32 v5, v2, 4, v3\n"
        "v_lshl_add_u32 v6, v2, 3, v3\n"
        "v_and_b32 v4, 127, v0\n"
        "v_lshlrev_b32 v12, 4, v4\n"
        "s_and_b32 s29, %1, 7\n"
        "s_lshr_b32 s30, %1, 3\n"
        "s_lshl_b32 s29, s29, 5\n"
        "s_add_u32 s29, s29, s30\n"
        "s_cmp_lt_u32 s29, 4\n"
        "s_cbranch_scc1 L%=_pathb\n"
        "s_sub_u32 s29, s29, 4\n"
        "s_mul_i32 s30, s29, 0xaaab\n"
        "s_lshr_b32 s30, s30, 17\n"
        "s_mul_i32 s31, s30, 3\n"
        "s_sub_u32 s49, s29, s31\n"
        "s_mul_i32 s48, s30, 110592\n"
        "s_mul_i32 s29, s30, 0xaaab\n"
        "s_lshr_b32 s29, s29, 17\n"
        "s_mul_i32 s29, s29, 3\n"
        "s_sub_u32 s50, s30, s29\n"
        "s_add_u32 s33, s48, 0\n"
        "s_add_u32 s34, s48, 36864\n"
        "s_add_u32 s35, s48, 73728\n"
        "s_cmp_ge_u32 s28, 2\n"
        "s_waitcnt lgkmcnt(0)\n"
        "s_cselect_b64 s[12:13], s[16:17], s[12:13]\n"
        "s_cmp_lg_u32 s47, 0\n"
        "s_cbranch_scc1 L%=_anogb\n"
        "buffer_load_dwordx4 v[26:29], v12, s[12:15], 0 offen\n"
        "L%=_anogb:\n"
        "buffer_load_dwordx4 v[32:35], v5, s[8:11], s33 offen offset:0 nt\n"
        "buffer_load_dwordx4 v[36:39], v5, s[8:11], s33 offen offset:256 nt\n"
        "buffer_load_dwordx4 v[40:43], v5, s[8:11], s33 offen offset:512 nt\n"
        "buffer_load_dwordx4 v[44:47], v5, s[8:11], s33 offen offset:768 nt\n"
        "buffer_load_dwordx2 v[48:49], v6, s[8:11], s33 offen offset:1024 nt\n"
        "v_bfe_u32 v3, v0, 7, 1\n"
        "v_lshl_add_u32 v7, v3, 12, v12\n"
        "v_lshlrev_b32 v8, 4, v2\n"
        "v_and_b32 v3, 1, v2\n"
        "v_lshlrev_b32 v3, 11, v3\n"
        "v_lshrrev_b32 v4, 1, v2\n"
        "v_lshl_add_u32 v9, v4, 4, v3\n"
        "v_and_b32 v1, 15, v1\n"
        "v_and_b32 v3, 7, v2\n"
        "v_xor_b32 v3, v3, v1\n"
        "v_lshlrev_b32 v3, 4, v3\n"
        "v_and_b32 v16, 3, v2\n"
        "v_lshl_add_u32 v3, v16, 8, v3\n"
        "v_lshrrev_b32 v16, 2, v2\n"
        "s_lshl_b32 s29, s47, 11\n"
        "s_lshl_b32 s30, s47, 10\n"
        "s_add_u32 s30, s30, 73728\n"
        "v_lshl_add_u32 v10, v16, 12, v3\n"
        "v_add_u32 v10, s29, v10\n"
        "v_lshl_add_u32 v13, v16, 11, v3\n"
        "v_add_u32 v13, s30, v13\n"
        "v_xor_b32 v3, v4, v1\n"
        "v_lshlrev_b32 v3, 4, v3\n"
        "v_and_b32 v16, 3, v4\n"
        "v_lshl_add_u32 v3, v16, 8, v3\n"
        "v_and_b32 v16, 1, v2\n"
        "v_lshl_add_u32 v3, v16, 3, v3\n"
        "v_lshrrev_b32 v16, 3, v2\n"
        "s_add_u32 s29, s29, 65536\n"
        "s_add_u32 s30, s30, 32768\n"
        "v_lshl_add_u32 v11, v16, 12, v3\n"
        "v_add_u32 v11, s29, v11\n"
        "v_lshl_add_u32 v19, v16, 11, v3\n"
        "v_add_u32 v19, s30, v19\n"
        "v_bfe_u32 v3, v0, 3, 5\n"
        "v_and_b32 v4, 2, v3\n"
        "v_bfe_u32 v16, v3, 3, 1\n"
        "v_lshl_add_u32 v4, v16, 2, v4\n"
        "v_and_b32 v16, 7, v0\n"
        "v_xor_b32 v4, v4, v16\n"
        "v_mul_u32_u24 v3, 1152, v3\n"
        "v_lshl_add_u32 v14, v4, 4, v3\n"
        "v_mov_b32 v15, 0x3727c5ac\n"
        "s_add_u32 s29, s50, 0\n"
        "s_cmp_ge_u32 s29, 3\n"
        "s_cselect_b32 s30, 3, 0\n"
        "s_sub_u32 s29, s29, s30\n"
        "s_mul_i32 s30, s49, 3\n"
        "s_add_u32 s29, s29, s30\n"
        "s_lshl_b32 s36, s29, 7\n"
        "s_add_u32 s29, s50, 1\n"
        "s_cmp_ge_u32 s29, 3\n"
        "s_cselect_b32 s30, 3, 0\n"
        "s_sub_u32 s29, s29, s30\n"
        "s_mul_i32 s30, s49, 3\n"
        "s_add_u32 s29, s29, s30\n"
        "s_lshl_b32 s37, s29, 7\n"
        "s_add_u32 s29, s50, 2\n"
        "s_cmp_ge_u32 s29, 3\n"
        "s_cselect_b32 s30, 3, 0\n"
        "s_sub_u32 s29, s29, s30\n"
        "s_mul_i32 s30, s49, 3\n"
        "s_add_u32 s29, s29, s30\n"
        "s_lshl_b32 s38, s29, 7\n"
        "s_mov_b32 s22, 663552\n"
        "s_mov_b32 s23, 0x20000\n"
        "s_movk_i32 s41, 0x3f80\n"
        "s_mov_b32 s42, 1.0\n"
        "s_mov_b32 s43, 0x3f803f80\n"
        "s_mov_b32 s44, 0xbae38e39\n"
        "s_mov_b32 s45, 0x3ae38e39\n"
        "s_lshl_b32 s46, s46, 10\n"
        "s_lshl_b32 s29, s47, 12\n"
        "s_add_u32 s52, s29, s46\n"
        "s_mul_i32 s53, s47, 36864\n"
        "s_add_u32 s53, s53, s36\n"
        "v_add_u32 v7, 110592, v7\n"
        "v_add_u32 v8, 110592, v8\n"
        "v_add_u32 v9, 110592, v9\n"
        "s_cmp_lg_u32 s47, 0\n"
        "s_cbranch_scc1 L%=_alate\n"
        "v_bfe_u32 v1, v0, 4, 2\n"
        "v_bfe_u32 v3, v0, 2, 2\n"
        "v_and_b32 v4, 3, v0\n"
        "v_lshl_add_u32 v16, v1, 3, v3\n"
        "v_lshlrev_b32 v236, 7, v16\n"
        "v_and_b32 v16, 1, v4\n"
        "v_lshl_add_u32 v236, v16, 3, v236\n"
        "v_and_b32 v3, 2, v3\n"
        "v_and_b32 v17, 1, v1\n"
        "v_lshl_add_u32 v3, v17, 2, v3\n"
        "v_bfe_u32 v16, v0, 7, 1\n"
        "v_lshrrev_b32 v4, 1, v4\n"
        "v_lshl_add_u32 v4, v16, 2, v4\n"
        "v_xor_b32 v4, v4, v3\n"
        "v_lshl_add_u32 v236, v4, 4, v236\n"
        "v_xor_b32 v237, 32, v236\n"
        "v_add_u32 v238, 65536, v236\n"
        "v_add_u32 v239, 65536, v237\n"
        "v_add_u32 v240, 131072, v236\n"
        "v_add_u32 v241, 131072, v237\n"
        "v_bfe_u32 v3, v0, 6, 1\n"
        "v_xor_b32 v4, v2, v1\n"
        "v_lshlrev_b32 v4, 4, v4\n"
        "v_lshl_add_u32 v4, v1, 8, v4\n"
        "v_lshl_add_u32 v242, v3, 11, v4\n"
        "v_xor_b32 v243, 64, v242\n"
        "v_add_u32 v244, 0x10000, v242\n"
        "v_add_u32 v245, 0x10000, v243\n"
        "v_lshl_add_u32 v246, v3, 10, v4\n"
        "v_add_u32 v246, 73728, v246\n"
        "v_xor_b32 v247, 64, v246\n"
        "v_lshl_add_u32 v3, v3, 4, v2\n"
        "v_lshrrev_b32 v4, 1, v1\n"
        "v_lshl_add_u32 v4, v16, 2, v4\n"
        "v_and_b32 v16, 7, v2\n"
        "v_xor_b32 v4, v4, v16\n"
        "v_lshlrev_b32 v4, 4, v4\n"
        "v_lshl_add_u32 v4, v3, 7, v4\n"
        "v_lshl_add_u32 v30, v17, 3, v4\n"
        "v_xor_b32 v31, 32, v30\n"
        "s_waitcnt vmcnt(5)\n"
        "v_lshlrev_b32 v196, 16, v26\n"
        "v_and_b32 v197, 0xffff0000, v26\n"
        "v_lshlrev_b32 v198, 16, v27\n"
        "v_and_b32 v199, 0xffff0000, v27\n"
        "v_lshlrev_b32 v200, 16, v28\n"
        "v_and_b32 v201, 0xffff0000, v28\n"
        "v_lshlrev_b32 v202, 16, v29\n"
        "v_and_b32 v203, 0xffff0000, v29\n"
        "ds_write_b128 v7, v[196:199]\n"
        "ds_write_b128 v7, v[200:203] offset:2048\n"
        "s_waitcnt lgkmcnt(0)\n"
        "s_branch L%=_aln\n"
        "L%=_alate:\n"
        "L%=_aln:\n"
        "s_barrier\n"
        "buffer_load_dwordx4 v[50:53], v5, s[8:11], s34 offen offset:0 nt\n"
        "buffer_load_dwordx4 v[54:57], v5, s[8:11], s34 offen offset:256 nt\n"
        "buffer_load_dwordx4 v[58:61], v5, s[8:11], s34 offen offset:512 nt\n"
        "buffer_load_dwordx4 v[62:65], v5, s[8:11], s34 offen offset:768 nt\n"
        "buffer_load_dwordx2 v[66:67], v6, s[8:11], s34 offen offset:1024 nt\n"
        "s_waitcnt vmcnt(5)\n"
        "s_setprio 3\n"
        "v_mov_b32 v16, 0\n"
        "v_mov_b32 v17, 0\n"
        "v_dot2c_f32_bf16 v16, s43, v32\n"
        "v_dot2c_f32_bf16 v17, s43, v33\n"
        "v_dot2c_f32_bf16 v16, s43, v34\n"
        "v_dot2c_f32_bf16 v17, s43, v35\n"
        "v_dot2c_f32_bf16 v16, s43, v36\n"
        "v_dot2c_f32_bf16 v17, s43, v37\n"
        "v_dot2c_f32_bf16 v16, s43, v38\n"
        "v_dot2c_f32_bf16 v17, s43, v39\n"
        "v_dot2c_f32_bf16 v16, s43, v40\n"
        "v_dot2c_f32_bf16 v17, s43, v41\n"
        "v_dot2c_f32_bf16 v16, s43, v42\n"
        "v_dot2c_f32_bf16 v17, s43, v43\n"
        "v_dot2c_f32_bf16 v16, s43, v44\n"
        "v_dot2c_f32_bf16 v17, s43, v45\n"
        "v_dot2c_f32_bf16 v16, s43, v46\n"
        "v_dot2c_f32_bf16 v17, s43, v47\n"
        "v_dot2c_f32_bf16 v16, s43, v48\n"
        "v_dot2c_f32_bf16 v17, s43, v49\n"
        "buffer_load_dwordx4 v[68:71], v5, s[8:11], s35 offen offset:0 nt\n"
        "buffer_load_dwordx4 v[72:75], v5, s[8:11], s35 offen offset:256 nt\n"
        "buffer_load_dwordx4 v[76:79], v5, s[8:11], s35 offen offset:512 nt\n"
        "buffer_load_dwordx4 v[80:83], v5, s[8:11], s35 offen offset:768 nt\n"
        "buffer_load_dwordx2 v[84:85], v6, s[8:11], s35 offen offset:1024 nt\n"
        "v_add_f32 v18, v16, v17\n"
        "s_nop 1\n"
        "v_add_f32_dpp v18, v18, v18 row_ror:8 row_mask:0xf bank_mask:0xf\n"
        "s_nop 1\n"
        "v_add_f32_dpp v18, v18, v18 row_ror:4 row_mask:0xf bank_mask:0xf\n"
        "s_nop 1\n"
        "v_add_f32_dpp v18, v18, v18 row_ror:2 row_mask:0xf bank_mask:0xf\n"
        "s_nop 1\n"
        "v_add_f32_dpp v18, v18, v18 row_ror:1 row_mask:0xf bank_mask:0xf\n"
        "v_mul_f32 v18, s44, v18\n"
        "v_cvt_pk_bf16_f32 v205, 0, v18\n"
        "v_sub_f32 v204, v18, v205\n"
        "v_mov_b32 v18, v205\n"
        "v_mul_f32 v204, v204, v204\n"
        "v_dot2_f32_bf16 v158, v32, s41, v18\n"
        "v_dot2_f32_bf16 v159, v32, s42, v18\n"
        "ds_read_b128 v[86:89], v8 offset:0\n"
        "v_dot2_f32_bf16 v160, v33, s41, v18\n"
        "v_dot2_f32_bf16 v161, v33, s42, v18\n"
        "ds_read_b128 v[90:93], v8 offset:2048\n"
        "v_dot2_f32_bf16 v162, v34, s41, v18\n"
        "v_dot2_f32_bf16 v163, v34, s42, v18\n"
        "ds_read_b128 v[94:97], v8 offset:256\n"
        "v_dot2_f32_bf16 v164, v35, s41, v18\n"
        "v_dot2_f32_bf16 v165, v35, s42, v18\n"
        "ds_read_b128 v[98:101], v8 offset:2304\n"
        "v_dot2_f32_bf16 v166, v36, s41, v18\n"
        "v_dot2_f32_bf16 v167, v36, s42, v18\n"
        "ds_read_b128 v[102:105], v8 offset:512\n"
        "v_dot2_f32_bf16 v168, v37, s41, v18\n"
        "v_dot2_f32_bf16 v169, v37, s42, v18\n"
        "ds_read_b128 v[106:109], v8 offset:2560\n"
        "v_dot2_f32_bf16 v170, v38, s41, v18\n"
        "v_dot2_f32_bf16 v171, v38, s42, v18\n"
        "ds_read_b128 v[110:113], v8 offset:768\n"
        "v_dot2_f32_bf16 v172, v39, s41, v18\n"
        "v_dot2_f32_bf16 v173, v39, s42, v18\n"
        "ds_read_b128 v[114:117], v8 offset:2816\n"
        "v_dot2_f32_bf16 v174, v40, s41, v18\n"
        "v_dot2_f32_bf16 v175, v40, s42, v18\n"
        "ds_read_b128 v[118:121], v9 offset:1024\n"
        "v_dot2_f32_bf16 v176, v41, s41, v18\n"
        "v_dot2_f32_bf16 v177, v41, s42, v18\n"
        "ds_read_b128 v[122:125], v8 offset:4096\n"
        "v_dot2_f32_bf16 v178, v42, s41, v18\n"
        "v_dot2_f32_bf16 v179, v42, s42, v18\n"
        "ds_read_b128 v[126:129], v8 offset:6144\n"
        "v_dot2_f32_bf16 v180, v43, s41, v18\n"
        "v_dot2_f32_bf16 v181, v43, s42, v18\n"
        "ds_read_b128 v[130:133], v8 offset:4352\n"
        "v_dot2_f32_bf16 v182, v44, s41, v18\n"
        "v_dot2_f32_bf16 v183, v44, s42, v18\n"
        "ds_read_b128 v[134:137], v8 offset:6400\n"
        "v_dot2_f32_bf16 v184, v45, s41, v18\n"
        "v_dot2_f32_bf16 v185, v45, s42, v18\n"
        "ds_read_b128 v[138:141], v8 offset:4608\n"
        "v_dot2_f32_bf16 v186, v46, s41, v18\n"
        "v_dot2_f32_bf16 v187, v46, s42, v18\n"
        "ds_read_b128 v[142:145], v8 offset:6656\n"
        "v_dot2_f32_bf16 v188, v47, s41, v18\n"
        "v_dot2_f32_bf16 v189, v47, s42, v18\n"
        "ds_read_b128 v[146:149], v8 offset:4864\n"
        "v_dot2_f32_bf16 v190, v48, s41, v18\n"
        "v_dot2_f32_bf16 v191, v48, s42, v18\n"
        "ds_read_b128 v[150:153], v8 offset:6912\n"
        "v_dot2_f32_bf16 v192, v49, s41, v18\n"
        "v_dot2_f32_bf16 v193, v49, s42, v18\n"
        "ds_read_b128 v[154:157], v9 offset:5120\n"
        "v_pk_mul_f32 v[22:23], v[158:159], v[158:159]\n"
        "v_pk_mul_f32 v[24:25], v[160:161], v[160:161]\n"
        "v_pk_fma_f32 v[22:23], v[162:163], v[162:163], v[22:23]\n"
        "v_pk_fma_f32 v[24:25], v[164:165], v[164:165], v[24:25]\n"
        "v_pk_fma_f32 v[22:23], v[166:167], v[166:167], v[22:23]\n"
        "v_pk_fma_f32 v[24:25], v[168:169], v[168:169], v[24:25]\n"
        "v_pk_fma_f32 v[22:23], v[170:171], v[170:171], v[22:23]\n"
        "v_pk_fma_f32 v[24:25], v[172:173], v[172:173], v[24:25]\n"
        "v_pk_fma_f32 v[22:23], v[174:175], v[174:175], v[22:23]\n"
        "v_pk_fma_f32 v[24:25], v[176:177], v[176:177], v[24:25]\n"
        "v_pk_fma_f32 v[22:23], v[178:179], v[178:179], v[22:23]\n"
        "v_pk_fma_f32 v[24:25], v[180:181], v[180:181], v[24:25]\n"
        "v_pk_fma_f32 v[22:23], v[182:183], v[182:183], v[22:23]\n"
        "v_pk_fma_f32 v[24:25], v[184:185], v[184:185], v[24:25]\n"
        "v_pk_fma_f32 v[22:23], v[186:187], v[186:187], v[22:23]\n"
        "v_pk_fma_f32 v[24:25], v[188:189], v[188:189], v[24:25]\n"
        "v_pk_fma_f32 v[22:23], v[190:191], v[190:191], v[22:23]\n"
        "v_pk_fma_f32 v[24:25], v[192:193], v[192:193], v[24:25]\n"
        "s_waitcnt lgkmcnt(0)\n"
        "v_pk_mul_f32 v[158:159], v[158:159], v[86:87]\n"
        "v_pk_mul_f32 v[160:161], v[160:161], v[88:89]\n"
        "v_pk_add_f32 v[22:23], v[22:23], v[24:25]\n"
        "v_pk_mul_f32 v[162:163], v[162:163], v[90:91]\n"
        "v_pk_mul_f32 v[164:165], v[164:165], v[92:93]\n"
        "v_add_f32 v20, v22, v23\n"
        "v_pk_mul_f32 v[166:167], v[166:167], v[94:95]\n"
        "v_pk_mul_f32 v[168:169], v[168:169], v[96:97]\n"
        "v_add_f32_dpp v20, v20, v20 row_ror:8 row_mask:0xf bank_mask:0xf\n"
        "v_pk_mul_f32 v[170:171], v[170:171], v[98:99]\n"
        "v_pk_mul_f32 v[172:173], v[172:173], v[100:101]\n"
        "v_add_f32_dpp v20, v20, v20 row_ror:4 row_mask:0xf bank_mask:0xf\n"
        "v_pk_mul_f32 v[174:175], v[174:175], v[102:103]\n"
        "v_pk_mul_f32 v[176:177], v[176:177], v[104:105]\n"
        "v_add_f32_dpp v20, v20, v20 row_ror:2 row_mask:0xf bank_mask:0xf\n"
        "v_pk_mul_f32 v[178:179], v[178:179], v[106:107]\n"
        "v_pk_mul_f32 v[180:181], v[180:181], v[108:109]\n"
        "v_add_f32_dpp v20, v20, v20 row_ror:1 row_mask:0xf bank_mask:0xf\n"
        "v_pk_mul_f32 v[182:183], v[182:183], v[110:111]\n"
        "v_fma_f32 v20, v20, s45, -v204\n"
        "v_pk_mul_f32 v[184:185], v[184:185], v[112:113]\n"
        "v_max_f32 v20, 0, v20\n"
        "v_pk_mul_f32 v[186:187], v[186:187], v[114:115]\n"
        "v_cvt_pk_bf16_f32 v20, 0, v20\n"
        "v_pk_mul_f32 v[188:189], v[188:189], v[116:117]\n"
        "v_add_f32 v20, v20, v15\n"
        "v_pk_mul_f32 v[190:191], v[190:191], v[118:119]\n"
        "v_cvt_pk_bf16_f32 v20, 0, v20\n"
        "v_pk_mul_f32 v[192:193], v[192:193], v[120:121]\n"
        "v_sqrt_f32 v20, v20\n"
        "s_nop 0\n"
        "v_cvt_pk_bf16_f32 v20, 0, v20\n"
        "v_rcp_f32 v20, v20\n"
        "s_waitcnt vmcnt(5)\n"
        "v_mov_b32 v16, 0\n"
        "v_mov_b32 v17, 0\n"
        "v_dot2c_f32_bf16 v16, s43, v50\n"
        "v_dot2c_f32_bf16 v17, s43, v51\n"
        "v_dot2c_f32_bf16 v16, s43, v52\n"
        "v_dot2c_f32_bf16 v17, s43, v53\n"
        "v_dot2c_f32_bf16 v16, s43, v54\n"
        "v_dot2c_f32_bf16 v17, s43, v55\n"
        "v_dot2c_f32_bf16 v16, s43, v56\n"
        "v_dot2c_f32_bf16 v17, s43, v57\n"
        "v_dot2c_f32_bf16 v16, s43, v58\n"
        "v_dot2c_f32_bf16 v17, s43, v59\n"
        "v_dot2c_f32_bf16 v16, s43, v60\n"
        "v_dot2c_f32_bf16 v17, s43, v61\n"
        "v_dot2c_f32_bf16 v16, s43, v62\n"
        "v_dot2c_f32_bf16 v17, s43, v63\n"
        "v_dot2c_f32_bf16 v16, s43, v64\n"
        "v_dot2c_f32_bf16 v17, s43, v65\n"
        "v_dot2c_f32_bf16 v16, s43, v66\n"
        "v_dot2c_f32_bf16 v17, s43, v67\n"
        "v_pk_fma_f32 v[158:159], v[158:159], v[20:21], v[122:123] op_sel_hi:[1,0,1]\n"
        "v_pk_fma_f32 v[160:161], v[160:161], v[20:21], v[124:125] op_sel_hi:[1,0,1]\n"
        "v_pk_fma_f32 v[162:163], v[162:163], v[20:21], v[126:127] op_sel_hi:[1,0,1]\n"
        "v_cvt_pk_bf16_f32 v32, v158, v159\n"
        "v_pk_fma_f32 v[164:165], v[164:165], v[20:21], v[128:129] op_sel_hi:[1,0,1]\n"
        "v_cvt_pk_bf16_f32 v33, v160, v161\n"
        "v_add_f32 v18, v16, v17\n"
        "v_pk_fma_f32 v[166:167], v[166:167], v[20:21], v[130:131] op_sel_hi:[1,0,1]\n"
        "v_cvt_pk_bf16_f32 v34, v162, v163\n"
        "v_pk_fma_f32 v[168:169], v[168:169], v[20:21], v[132:133] op_sel_hi:[1,0,1]\n"
        "v_add_f32_dpp v18, v18, v18 row_ror:8 row_mask:0xf bank_mask:0xf\n"
        "v_cvt_pk_bf16_f32 v35, v164, v165\n"
        "ds_write_b128 v10, v[32:35] offset:0\n"
        "v_pk_fma_f32 v[170:171], v[170:171], v[20:21], v[134:135] op_sel_hi:[1,0,1]\n"
        "v_add_f32_dpp v18, v18, v18 row_ror:4 row_mask:0xf bank_mask:0xf\n"
        "v_cvt_pk_bf16_f32 v36, v166, v167\n"
        "v_pk_fma_f32 v[172:173], v[172:173], v[20:21], v[136:137] op_sel_hi:[1,0,1]\n"
        "v_cvt_pk_bf16_f32 v37, v168, v169\n"
        "v_add_f32_dpp v18, v18, v18 row_ror:2 row_mask:0xf bank_mask:0xf\n"
        "v_pk_fma_f32 v[174:175], v[174:175], v[20:21], v[138:139] op_sel_hi:[1,0,1]\n"
        "v_cvt_pk_bf16_f32 v38, v170, v171\n"
        "v_pk_fma_f32 v[176:177], v[176:177], v[20:21], v[140:141] op_sel_hi:[1,0,1]\n"
        "v_add_f32_dpp v18, v18, v18 row_ror:1 row_mask:0xf bank_mask:0xf\n"
        "v_cvt_pk_bf16_f32 v39, v172, v173\n"
        "ds_write_b128 v10, v[36:39] offset:16384\n"
        "v_pk_fma_f32 v[178:179], v[178:179], v[20:21], v[142:143] op_sel_hi:[1,0,1]\n"
        "v_mul_f32 v18, s44, v18\n"
        "v_cvt_pk_bf16_f32 v40, v174, v175\n"
        "v_pk_fma_f32 v[180:181], v[180:181], v[20:21], v[144:145] op_sel_hi:[1,0,1]\n"
        "v_cvt_pk_bf16_f32 v41, v176, v177\n"
        "v_cvt_pk_bf16_f32 v205, 0, v18\n"
        "v_pk_fma_f32 v[182:183], v[182:183], v[20:21], v[146:147] op_sel_hi:[1,0,1]\n"
        "v_cvt_pk_bf16_f32 v42, v178, v179\n"
        "v_pk_fma_f32 v[184:185], v[184:185], v[20:21], v[148:149] op_sel_hi:[1,0,1]\n"
        "v_sub_f32 v204, v18, v205\n"
        "v_cvt_pk_bf16_f32 v43, v180, v181\n"
        "ds_write_b128 v10, v[40:43] offset:32768\n"
        "v_pk_fma_f32 v[186:187], v[186:187], v[20:21], v[150:151] op_sel_hi:[1,0,1]\n"
        "v_mov_b32 v18, v205\n"
        "v_cvt_pk_bf16_f32 v44, v182, v183\n"
        "v_pk_fma_f32 v[188:189], v[188:189], v[20:21], v[152:153] op_sel_hi:[1,0,1]\n"
        "v_cvt_pk_bf16_f32 v45, v184, v185\n"
        "v_mul_f32 v204, v204, v204\n"
        "v_pk_fma_f32 v[190:191], v[190:191], v[20:21], v[154:155] op_sel_hi:[1,0,1]\n"
        "v_cvt_pk_bf16_f32 v46, v186, v187\n"
        "v_pk_fma_f32 v[192:193], v[192:193], v[20:21], v[156:157] op_sel_hi:[1,0,1]\n"
        "v_cvt_pk_bf16_f32 v47, v188, v189\n"
        "ds_write_b128 v10, v[44:47] offset:49152\n"
        "v_cvt_pk_bf16_f32 v48, v190, v191\n"
        "v_cvt_pk_bf16_f32 v49, v192, v193\n"
        "ds_write_b64 v11, v[48:49] offset:0\n"
        "s_setprio 2\n"
        "v_dot2_f32_bf16 v158, v50, s41, v18\n"
        "v_dot2_f32_bf16 v159, v50, s42, v18\n"
        "v_dot2_f32_bf16 v160, v51, s41, v18\n"
        "v_dot2_f32_bf16 v161, v51, s42, v18\n"
        "v_dot2_f32_bf16 v162, v52, s41, v18\n"
        "v_dot2_f32_bf16 v163, v52, s42, v18\n"
        "v_dot2_f32_bf16 v164, v53, s41, v18\n"
        "v_dot2_f32_bf16 v165, v53, s42, v18\n"
        "v_dot2_f32_bf16 v166, v54, s41, v18\n"
        "v_dot2_f32_bf16 v167, v54, s42, v18\n"
        "v_dot2_f32_bf16 v168, v55, s41, v18\n"
        "v_dot2_f32_bf16 v169, v55, s42, v18\n"
        "v_dot2_f32_bf16 v170, v56, s41, v18\n"
        "v_dot2_f32_bf16 v171, v56, s42, v18\n"
        "v_dot2_f32_bf16 v172, v57, s41, v18\n"
        "v_dot2_f32_bf16 v173, v57, s42, v18\n"
        "v_dot2_f32_bf16 v174, v58, s41, v18\n"
        "v_dot2_f32_bf16 v175, v58, s42, v18\n"
        "v_dot2_f32_bf16 v176, v59, s41, v18\n"
        "v_dot2_f32_bf16 v177, v59, s42, v18\n"
        "v_dot2_f32_bf16 v178, v60, s41, v18\n"
        "v_dot2_f32_bf16 v179, v60, s42, v18\n"
        "v_dot2_f32_bf16 v180, v61, s41, v18\n"
        "v_dot2_f32_bf16 v181, v61, s42, v18\n"
        "v_dot2_f32_bf16 v182, v62, s41, v18\n"
        "v_dot2_f32_bf16 v183, v62, s42, v18\n"
        "v_dot2_f32_bf16 v184, v63, s41, v18\n"
        "s_add_u32 m0, s52, 118784\n"
        "s_add_u32 s51, s53, 0\n"
        "buffer_load_dwordx4 v14, s[20:23], s51 offen lds\n"
        "v_dot2_f32_bf16 v185, v63, s42, v18\n"
        "v_dot2_f32_bf16 v186, v64, s41, v18\n"
        "v_dot2_f32_bf16 v187, v64, s42, v18\n"
        "v_dot2_f32_bf16 v188, v65, s41, v18\n"
        "v_dot2_f32_bf16 v189, v65, s42, v18\n"
        "v_dot2_f32_bf16 v190, v66, s41, v18\n"
        "v_dot2_f32_bf16 v191, v66, s42, v18\n"
        "v_dot2_f32_bf16 v192, v67, s41, v18\n"
        "v_dot2_f32_bf16 v193, v67, s42, v18\n"
        "v_pk_mul_f32 v[22:23], v[158:159], v[158:159]\n"
        "v_pk_mul_f32 v[24:25], v[160:161], v[160:161]\n"
        "v_pk_fma_f32 v[22:23], v[162:163], v[162:163], v[22:23]\n"
        "v_pk_fma_f32 v[24:25], v[164:165], v[164:165], v[24:25]\n"
        "v_pk_fma_f32 v[22:23], v[166:167], v[166:167], v[22:23]\n"
        "v_pk_fma_f32 v[24:25], v[168:169], v[168:169], v[24:25]\n"
        "v_pk_fma_f32 v[22:23], v[170:171], v[170:171], v[22:23]\n"
        "v_pk_fma_f32 v[24:25], v[172:173], v[172:173], v[24:25]\n"
        "v_pk_fma_f32 v[22:23], v[174:175], v[174:175], v[22:23]\n"
        "v_pk_fma_f32 v[24:25], v[176:177], v[176:177], v[24:25]\n"
        "v_pk_fma_f32 v[22:23], v[178:179], v[178:179], v[22:23]\n"
        "v_pk_fma_f32 v[24:25], v[180:181], v[180:181], v[24:25]\n"
        "v_pk_fma_f32 v[22:23], v[182:183], v[182:183], v[22:23]\n"
        "v_pk_fma_f32 v[24:25], v[184:185], v[184:185], v[24:25]\n"
        "v_pk_fma_f32 v[22:23], v[186:187], v[186:187], v[22:23]\n"
        "v_pk_fma_f32 v[24:25], v[188:189], v[188:189], v[24:25]\n"
        "s_add_u32 m0, s52, 126976\n"
        "s_add_u32 s51, s53, 73728\n"
        "buffer_load_dwordx4 v14, s[20:23], s51 offen lds\n"
        "v_pk_fma_f32 v[22:23], v[190:191], v[190:191], v[22:23]\n"
        "v_pk_fma_f32 v[24:25], v[192:193], v[192:193], v[24:25]\n"
        "v_pk_mul_f32 v[158:159], v[158:159], v[86:87]\n"
        "v_pk_mul_f32 v[160:161], v[160:161], v[88:89]\n"
        "v_pk_mul_f32 v[162:163], v[162:163], v[90:91]\n"
        "v_pk_add_f32 v[22:23], v[22:23], v[24:25]\n"
        "v_pk_mul_f32 v[164:165], v[164:165], v[92:93]\n"
        "v_pk_mul_f32 v[166:167], v[166:167], v[94:95]\n"
        "v_add_f32 v20, v22, v23\n"
        "v_pk_mul_f32 v[168:169], v[168:169], v[96:97]\n"
        "v_pk_mul_f32 v[170:171], v[170:171], v[98:99]\n"
        "v_add_f32_dpp v20, v20, v20 row_ror:8 row_mask:0xf bank_mask:0xf\n"
        "v_pk_mul_f32 v[172:173], v[172:173], v[100:101]\n"
        "v_pk_mul_f32 v[174:175], v[174:175], v[102:103]\n"
        "v_add_f32_dpp v20, v20, v20 row_ror:4 row_mask:0xf bank_mask:0xf\n"
        "v_pk_mul_f32 v[176:177], v[176:177], v[104:105]\n"
        "v_pk_mul_f32 v[178:179], v[178:179], v[106:107]\n"
        "v_add_f32_dpp v20, v20, v20 row_ror:2 row_mask:0xf bank_mask:0xf\n"
        "v_pk_mul_f32 v[180:181], v[180:181], v[108:109]\n"
        "v_pk_mul_f32 v[182:183], v[182:183], v[110:111]\n"
        "v_add_f32_dpp v20, v20, v20 row_ror:1 row_mask:0xf bank_mask:0xf\n"
        "v_pk_mul_f32 v[184:185], v[184:185], v[112:113]\n"
        "v_fma_f32 v20, v20, s45, -v204\n"
        "v_pk_mul_f32 v[186:187], v[186:187], v[114:115]\n"
        "v_max_f32 v20, 0, v20\n"
        "v_pk_mul_f32 v[188:189], v[188:189], v[116:117]\n"
        "v_cvt_pk_bf16_f32 v20, 0, v20\n"
        "v_pk_mul_f32 v[190:191], v[190:191], v[118:119]\n"
        "v_add_f32 v20, v20, v15\n"
        "v_pk_mul_f32 v[192:193], v[192:193], v[120:121]\n"
        "v_cvt_pk_bf16_f32 v20, 0, v20\n"
        "v_sqrt_f32 v20, v20\n"
        "s_nop 0\n"
        "v_cvt_pk_bf16_f32 v20, 0, v20\n"
        "v_rcp_f32 v20, v20\n"
        "s_waitcnt vmcnt(2)\n"
        "v_mov_b32 v16, 0\n"
        "v_mov_b32 v17, 0\n"
        "v_dot2c_f32_bf16 v16, s43, v68\n"
        "v_dot2c_f32_bf16 v17, s43, v69\n"
        "v_dot2c_f32_bf16 v16, s43, v70\n"
        "v_dot2c_f32_bf16 v17, s43, v71\n"
        "v_dot2c_f32_bf16 v16, s43, v72\n"
        "v_dot2c_f32_bf16 v17, s43, v73\n"
        "v_dot2c_f32_bf16 v16, s43, v74\n"
        "v_dot2c_f32_bf16 v17, s43, v75\n"
        "v_dot2c_f32_bf16 v16, s43, v76\n"
        "v_dot2c_f32_bf16 v17, s43, v77\n"
        "v_dot2c_f32_bf16 v16, s43, v78\n"
        "v_dot2c_f32_bf16 v17, s43, v79\n"
        "v_dot2c_f32_bf16 v16, s43, v80\n"
        "v_dot2c_f32_bf16 v17, s43, v81\n"
        "v_dot2c_f32_bf16 v16, s43, v82\n"
        "v_dot2c_f32_bf16 v17, s43, v83\n"
        "v_dot2c_f32_bf16 v16, s43, v84\n"
        "v_dot2c_f32_bf16 v17, s43, v85\n"
        "v_pk_fma_f32 v[158:159], v[158:159], v[20:21], v[122:123] op_sel_hi:[1,0,1]\n"
        "v_pk_fma_f32 v[160:161], v[160:161], v[20:21], v[124:125] op_sel_hi:[1,0,1]\n"
        "v_pk_fma_f32 v[162:163], v[162:163], v[20:21], v[126:127] op_sel_hi:[1,0,1]\n"
        "v_cvt_pk_bf16_f32 v50, v158, v159\n"
        "v_pk_fma_f32 v[164:165], v[164:165], v[20:21], v[128:129] op_sel_hi:[1,0,1]\n"
        "v_cvt_pk_bf16_f32 v51, v160, v161\n"
        "v_add_f32 v18, v16, v17\n"
        "v_pk_fma_f32 v[166:167], v[166:167], v[20:21], v[130:131] op_sel_hi:[1,0,1]\n"
        "v_cvt_pk_bf16_f32 v52, v162, v163\n"
        "v_pk_fma_f32 v[168:169], v[168:169], v[20:21], v[132:133] op_sel_hi:[1,0,1]\n"
        "v_add_f32_dpp v18, v18, v18 row_ror:8 row_mask:0xf bank_mask:0xf\n"
        "v_cvt_pk_bf16_f32 v53, v164, v165\n"
        "ds_write_b128 v10, v[50:53] offset:1024\n"
        "v_pk_fma_f32 v[170:171], v[170:171], v[20:21], v[134:135] op_sel_hi:[1,0,1]\n"
        "v_add_f32_dpp v18, v18, v18 row_ror:4 row_mask:0xf bank_mask:0xf\n"
        "v_cvt_pk_bf16_f32 v54, v166, v167\n"
        "v_pk_fma_f32 v[172:173], v[172:173], v[20:21], v[136:137] op_sel_hi:[1,0,1]\n"
        "s_add_u32 m0, s52, 135168\n"
        "s_add_u32 s51, s53, 147456\n"
        "buffer_load_dwordx4 v14, s[20:23], s51 offen lds\n"
        "v_cvt_pk_bf16_f32 v55, v168, v169\n"
        "v_add_f32_dpp v18, v18, v18 row_ror:2 row_mask:0xf bank_mask:0xf\n"
        "v_pk_fma_f32 v[174:175], v[174:175], v[20:21], v[138:139] op_sel_hi:[1,0,1]\n"
        "v_cvt_pk_bf16_f32 v56, v170, v171\n"
        "v_pk_fma_f32 v[176:177], v[176:177], v[20:21], v[140:141] op_sel_hi:[1,0,1]\n"
        "v_add_f32_dpp v18, v18, v18 row_ror:1 row_mask:0xf bank_mask:0xf\n"
        "v_cvt_pk_bf16_f32 v57, v172, v173\n"
        "ds_write_b128 v10, v[54:57] offset:17408\n"
        "v_pk_fma_f32 v[178:179], v[178:179], v[20:21], v[142:143] op_sel_hi:[1,0,1]\n"
        "v_mul_f32 v18, s44, v18\n"
        "v_cvt_pk_bf16_f32 v58, v174, v175\n"
        "v_pk_fma_f32 v[180:181], v[180:181], v[20:21], v[144:145] op_sel_hi:[1,0,1]\n"
        "v_cvt_pk_bf16_f32 v59, v176, v177\n"
        "v_cvt_pk_bf16_f32 v205, 0, v18\n"
        "v_pk_fma_f32 v[182:183], v[182:183], v[20:21], v[146:147] op_sel_hi:[1,0,1]\n"
        "s_add_u32 m0, s52, 143360\n"
        "s_add_u32 s51, s53, 221184\n"
        "buffer_load_dwordx4 v14, s[20:23], s51 offen lds\n"
        "v_cvt_pk_bf16_f32 v60, v178, v179\n"
        "v_pk_fma_f32 v[184:185], v[184:185], v[20:21], v[148:149] op_sel_hi:[1,0,1]\n"
        "v_sub_f32 v204, v18, v205\n"
        "v_cvt_pk_bf16_f32 v61, v180, v181\n"
        "ds_write_b128 v10, v[58:61] offset:33792\n"
        "v_pk_fma_f32 v[186:187], v[186:187], v[20:21], v[150:151] op_sel_hi:[1,0,1]\n"
        "v_mov_b32 v18, v205\n"
        "v_cvt_pk_bf16_f32 v62, v182, v183\n"
        "v_pk_fma_f32 v[188:189], v[188:189], v[20:21], v[152:153] op_sel_hi:[1,0,1]\n"
        "v_cvt_pk_bf16_f32 v63, v184, v185\n"
        "v_mul_f32 v204, v204, v204\n"
        "v_pk_fma_f32 v[190:191], v[190:191], v[20:21], v[154:155] op_sel_hi:[1,0,1]\n"
        "v_cvt_pk_bf16_f32 v64, v186, v187\n"
        "v_pk_fma_f32 v[192:193], v[192:193], v[20:21], v[156:157] op_sel_hi:[1,0,1]\n"
        "v_cvt_pk_bf16_f32 v65, v188, v189\n"
        "s_add_u32 m0, s52, 151552\n"
        "s_add_u32 s51, s53, 294912\n"
        "buffer_load_dwordx4 v14, s[20:23], s51 offen lds\n"
        "ds_write_b128 v10, v[62:65] offset:50176\n"
        "v_cvt_pk_bf16_f32 v66, v190, v191\n"
        "v_cvt_pk_bf16_f32 v67, v192, v193\n"
        "ds_write_b64 v11, v[66:67] offset:1024\n"
        "s_setprio 1\n"
        "v_dot2_f32_bf16 v158, v68, s41, v18\n"
        "v_dot2_f32_bf16 v159, v68, s42, v18\n"
        "v_dot2_f32_bf16 v160, v69, s41, v18\n"
        "v_dot2_f32_bf16 v161, v69, s42, v18\n"
        "v_dot2_f32_bf16 v162, v70, s41, v18\n"
        "v_dot2_f32_bf16 v163, v70, s42, v18\n"
        "v_dot2_f32_bf16 v164, v71, s41, v18\n"
        "v_dot2_f32_bf16 v165, v71, s42, v18\n"
        "v_dot2_f32_bf16 v166, v72, s41, v18\n"
        "v_dot2_f32_bf16 v167, v72, s42, v18\n"
        "v_dot2_f32_bf16 v168, v73, s41, v18\n"
        "v_dot2_f32_bf16 v169, v73, s42, v18\n"
        "v_dot2_f32_bf16 v170, v74, s41, v18\n"
        "v_dot2_f32_bf16 v171, v74, s42, v18\n"
        "v_dot2_f32_bf16 v172, v75, s41, v18\n"
        "v_dot2_f32_bf16 v173, v75, s42, v18\n"
        "v_dot2_f32_bf16 v174, v76, s41, v18\n"
        "v_dot2_f32_bf16 v175, v76, s42, v18\n"
        "v_dot2_f32_bf16 v176, v77, s41, v18\n"
        "v_dot2_f32_bf16 v177, v77, s42, v18\n"
        "v_dot2_f32_bf16 v178, v78, s41, v18\n"
        "v_dot2_f32_bf16 v179, v78, s42, v18\n"
        "v_dot2_f32_bf16 v180, v79, s41, v18\n"
        "v_dot2_f32_bf16 v181, v79, s42, v18\n"
        "v_dot2_f32_bf16 v182, v80, s41, v18\n"
        "v_dot2_f32_bf16 v183, v80, s42, v18\n"
        "v_dot2_f32_bf16 v184, v81, s41, v18\n"
        "v_dot2_f32_bf16 v185, v81, s42, v18\n"
        "v_dot2_f32_bf16 v186, v82, s41, v18\n"
        "v_dot2_f32_bf16 v187, v82, s42, v18\n"
        "v_dot2_f32_bf16 v188, v83, s41, v18\n"
        "v_dot2_f32_bf16 v189, v83, s42, v18\n"
        "v_dot2_f32_bf16 v190, v84, s41, v18\n"
        "v_dot2_f32_bf16 v191, v84, s42, v18\n"
        "v_dot2_f32_bf16 v192, v85, s41, v18\n"
        "v_dot2_f32_bf16 v193, v85, s42, v18\n"
        "v_pk_mul_f32 v[22:23], v[158:159], v[158:159]\n"
        "v_pk_mul_f32 v[24:25], v[160:161], v[160:161]\n"
        "v_pk_fma_f32 v[22:23], v[162:163], v[162:163], v[22:23]\n"
        "v_pk_fma_f32 v[24:25], v[164:165], v[164:165], v[24:25]\n"
        "v_pk_fma_f32 v[22:23], v[166:167], v[166:167], v[22:23]\n"
        "v_pk_fma_f32 v[24:25], v[168:169], v[168:169], v[24:25]\n"
        "v_pk_fma_f32 v[22:23], v[170:171], v[170:171], v[22:23]\n"
        "v_pk_fma_f32 v[24:25], v[172:173], v[172:173], v[24:25]\n"
        "v_pk_fma_f32 v[22:23], v[174:175], v[174:175], v[22:23]\n"
        "v_pk_fma_f32 v[24:25], v[176:177], v[176:177], v[24:25]\n"
        "v_pk_fma_f32 v[22:23], v[178:179], v[178:179], v[22:23]\n"
        "v_pk_fma_f32 v[24:25], v[180:181], v[180:181], v[24:25]\n"
        "v_pk_fma_f32 v[22:23], v[182:183], v[182:183], v[22:23]\n"
        "v_pk_fma_f32 v[24:25], v[184:185], v[184:185], v[24:25]\n"
        "v_pk_fma_f32 v[22:23], v[186:187], v[186:187], v[22:23]\n"
        "v_pk_fma_f32 v[24:25], v[188:189], v[188:189], v[24:25]\n"
        "v_pk_fma_f32 v[22:23], v[190:191], v[190:191], v[22:23]\n"
        "v_pk_fma_f32 v[24:25], v[192:193], v[192:193], v[24:25]\n"
        "v_pk_mul_f32 v[158:159], v[158:159], v[86:87]\n"
        "v_pk_mul_f32 v[160:161], v[160:161], v[88:89]\n"
        "v_pk_mul_f32 v[162:163], v[162:163], v[90:91]\n"
        "v_pk_add_f32 v[22:23], v[22:23], v[24:25]\n"
        "v_pk_mul_f32 v[164:165], v[164:165], v[92:93]\n"
        "v_pk_mul_f32 v[166:167], v[166:167], v[94:95]\n"
        "v_add_f32 v20, v22, v23\n"
        "v_pk_mul_f32 v[168:169], v[168:169], v[96:97]\n"
        "v_pk_mul_f32 v[170:171], v[170:171], v[98:99]\n"
        "v_add_f32_dpp v20, v20, v20 row_ror:8 row_mask:0xf bank_mask:0xf\n"
        "v_pk_mul_f32 v[172:173], v[172:173], v[100:101]\n"
        "v_pk_mul_f32 v[174:175], v[174:175], v[102:103]\n"
        "v_add_f32_dpp v20, v20, v20 row_ror:4 row_mask:0xf bank_mask:0xf\n"
        "v_pk_mul_f32 v[176:177], v[176:177], v[104:105]\n"
        "v_pk_mul_f32 v[178:179], v[178:179], v[106:107]\n"
        "v_add_f32_dpp v20, v20, v20 row_ror:2 row_mask:0xf bank_mask:0xf\n"
        "v_pk_mul_f32 v[180:181], v[180:181], v[108:109]\n"
        "v_pk_mul_f32 v[182:183], v[182:183], v[110:111]\n"
        "v_add_f32_dpp v20, v20, v20 row_ror:1 row_mask:0xf bank_mask:0xf\n"
        "v_pk_mul_f32 v[184:185], v[184:185], v[112:113]\n"
        "v_fma_f32 v20, v20, s45, -v204\n"
        "v_pk_mul_f32 v[186:187], v[186:187], v[114:115]\n"
        "v_max_f32 v20, 0, v20\n"
        "v_pk_mul_f32 v[188:189], v[188:189], v[116:117]\n"
        "v_cvt_pk_bf16_f32 v20, 0, v20\n"
        "v_pk_mul_f32 v[190:191], v[190:191], v[118:119]\n"
        "v_add_f32 v20, v20, v15\n"
        "v_pk_mul_f32 v[192:193], v[192:193], v[120:121]\n"
        "v_cvt_pk_bf16_f32 v20, 0, v20\n"
        "v_sqrt_f32 v20, v20\n"
        "s_nop 0\n"
        "v_cvt_pk_bf16_f32 v20, 0, v20\n"
        "v_rcp_f32 v20, v20\n"
        "s_nop 0\n"
        "v_pk_fma_f32 v[158:159], v[158:159], v[20:21], v[122:123] op_sel_hi:[1,0,1]\n"
        "v_pk_fma_f32 v[160:161], v[160:161], v[20:21], v[124:125] op_sel_hi:[1,0,1]\n"
        "v_pk_fma_f32 v[162:163], v[162:163], v[20:21], v[126:127] op_sel_hi:[1,0,1]\n"
        "v_cvt_pk_bf16_f32 v68, v158, v159\n"
        "v_pk_fma_f32 v[164:165], v[164:165], v[20:21], v[128:129] op_sel_hi:[1,0,1]\n"
        "v_cvt_pk_bf16_f32 v69, v160, v161\n"
        "v_pk_fma_f32 v[166:167], v[166:167], v[20:21], v[130:131] op_sel_hi:[1,0,1]\n"
        "v_cvt_pk_bf16_f32 v70, v162, v163\n"
        "v_pk_fma_f32 v[168:169], v[168:169], v[20:21], v[132:133] op_sel_hi:[1,0,1]\n"
        "v_cvt_pk_bf16_f32 v71, v164, v165\n"
        "ds_write_b128 v13, v[68:71] offset:0\n"
        "v_pk_fma_f32 v[170:171], v[170:171], v[20:21], v[134:135] op_sel_hi:[1,0,1]\n"
        "v_cvt_pk_bf16_f32 v72, v166, v167\n"
        "v_pk_fma_f32 v[172:173], v[172:173], v[20:21], v[136:137] op_sel_hi:[1,0,1]\n"
        "v_cvt_pk_bf16_f32 v73, v168, v169\n"
        "v_pk_fma_f32 v[174:175], v[174:175], v[20:21], v[138:139] op_sel_hi:[1,0,1]\n"
        "v_cvt_pk_bf16_f32 v74, v170, v171\n"
        "v_pk_fma_f32 v[176:177], v[176:177], v[20:21], v[140:141] op_sel_hi:[1,0,1]\n"
        "v_cvt_pk_bf16_f32 v75, v172, v173\n"
        "ds_write_b128 v13, v[72:75] offset:8192\n"
        "v_pk_fma_f32 v[178:179], v[178:179], v[20:21], v[142:143] op_sel_hi:[1,0,1]\n"
        "v_cvt_pk_bf16_f32 v76, v174, v175\n"
        "v_pk_fma_f32 v[180:181], v[180:181], v[20:21], v[144:145] op_sel_hi:[1,0,1]\n"
        "v_cvt_pk_bf16_f32 v77, v176, v177\n"
        "v_pk_fma_f32 v[182:183], v[182:183], v[20:21], v[146:147] op_sel_hi:[1,0,1]\n"
        "v_cvt_pk_bf16_f32 v78, v178, v179\n"
        "v_pk_fma_f32 v[184:185], v[184:185], v[20:21], v[148:149] op_sel_hi:[1,0,1]\n"
        "v_cvt_pk_bf16_f32 v79, v180, v181\n"
        "ds_write_b128 v13, v[76:79] offset:16384\n"
        "v_pk_fma_f32 v[186:187], v[186:187], v[20:21], v[150:151] op_sel_hi:[1,0,1]\n"
        "v_cvt_pk_bf16_f32 v80, v182, v183\n"
        "v_pk_fma_f32 v[188:189], v[188:189], v[20:21], v[152:153] op_sel_hi:[1,0,1]\n"
        "v_cvt_pk_bf16_f32 v81, v184, v185\n"
        "v_pk_fma_f32 v[190:191], v[190:191], v[20:21], v[154:155] op_sel_hi:[1,0,1]\n"
        "v_cvt_pk_bf16_f32 v82, v186, v187\n"
        "v_pk_fma_f32 v[192:193], v[192:193], v[20:21], v[156:157] op_sel_hi:[1,0,1]\n"
        "v_cvt_pk_bf16_f32 v83, v188, v189\n"
        "ds_write_b128 v13, v[80:83] offset:24576\n"
        "v_cvt_pk_bf16_f32 v84, v190, v191\n"
        "v_cvt_pk_bf16_f32 v85, v192, v193\n"
        "ds_write_b64 v19, v[84:85] offset:0\n"
        "s_setprio 0\n"
        "s_cmp_lg_u32 s47, 0\n"
        "s_cbranch_scc1 L%=_ahelper\n"
        "s_waitcnt vmcnt(2) lgkmcnt(0)\n"
        "s_barrier\n"
        "s_setprio 1\n"
        "ds_read_b64_tr_b16 v[176:177], v238 offset:53248\n"
        "ds_read_b128 v[32:35], v242 offset:0\n"
        "ds_read_b64_tr_b16 v[178:179], v238 offset:53760\n"
        "ds_read_b64_tr_b16 v[180:181], v239 offset:53248\n"
        "ds_read_b128 v[104:107], v242 offset:1024\n"
        "ds_read_b64_tr_b16 v[182:183], v239 offset:53760\n"
        "ds_read_b128 v[200:203], v246 offset:0\n"
        "ds_read_b64_tr_b16 v[184:185], v238 offset:57344\n"
        "ds_read_b128 v[36:39], v243 offset:4096\n"
        "ds_read_b64_tr_b16 v[186:187], v238 offset:57856\n"
        "ds_read_b64_tr_b16 v[188:189], v239 offset:57344\n"
        "ds_read_b128 v[108:111], v243 offset:5120\n"
        "ds_read_b64_tr_b16 v[190:191], v239 offset:57856\n"
        "ds_read_b128 v[204:207], v247 offset:2048\n"
        "s_waitcnt lgkmcnt(7)\n"
        "v_mfma_f32_16x16x32_bf16 v[212:215], v[176:179], v[32:35], 0\n"
        "ds_read_b64_tr_b16 v[192:193], v238 offset:61440\n"
        "ds_read_b128 v[40:43], v242 offset:8192\n"
        "v_mfma_f32_16x16x32_bf16 v[216:219], v[180:183], v[32:35], 0\n"
        "ds_read_b64_tr_b16 v[194:195], v238 offset:61952\n"
        "v_mfma_f32_16x16x32_bf16 v[220:223], v[176:179], v[104:107], 0\n"
        "ds_read_b64_tr_b16 v[196:197], v239 offset:61440\n"
        "v_mfma_f32_16x16x32_bf16 v[224:227], v[180:183], v[104:107], 0\n"
        "ds_read_b128 v[112:115], v242 offset:9216\n"
        "v_mfma_f32_16x16x32_bf16 v[228:231], v[176:179], v[200:203], 0\n"
        "ds_read_b64_tr_b16 v[198:199], v239 offset:61952\n"
        "v_mfma_f32_16x16x32_bf16 v[232:235], v[180:183], v[200:203], 0\n"
        "ds_read_b128 v[208:211], v246 offset:4096\n"
        "s_waitcnt lgkmcnt(7)\n"
        "v_mfma_f32_16x16x32_bf16 v[212:215], v[184:187], v[36:39], v[212:215]\n"
        "ds_read_b64_tr_b16 v[176:177], v240 offset:0\n"
        "ds_read_b128 v[44:47], v243 offset:12288\n"
        "v_mfma_f32_16x16x32_bf16 v[216:219], v[188:191], v[36:39], v[216:219]\n"
        "ds_read_b64_tr_b16 v[178:179], v240 offset:512\n"
        "v_mfma_f32_16x16x32_bf16 v[220:223], v[184:187], v[108:111], v[220:223]\n"
        "ds_read_b64_tr_b16 v[180:181], v241 offset:0\n"
        "v_mfma_f32_16x16x32_bf16 v[224:227], v[188:191], v[108:111], v[224:227]\n"
        "ds_read_b128 v[116:119], v243 offset:13312\n"
        "v_mfma_f32_16x16x32_bf16 v[228:231], v[184:187], v[204:207], v[228:231]\n"
        "ds_read_b64_tr_b16 v[182:183], v241 offset:512\n"
        "v_mfma_f32_16x16x32_bf16 v[232:235], v[188:191], v[204:207], v[232:235]\n"
        "ds_read_b128 v[200:203], v247 offset:6144\n"
        "s_waitcnt lgkmcnt(7)\n"
        "v_mfma_f32_16x16x32_bf16 v[212:215], v[192:195], v[40:43], v[212:215]\n"
        "ds_read_b64_tr_b16 v[184:185], v240 offset:4096\n"
        "ds_read_b128 v[48:51], v242 offset:16384\n"
        "v_mfma_f32_16x16x32_bf16 v[216:219], v[196:199], v[40:43], v[216:219]\n"
        "ds_read_b64_tr_b16 v[186:187], v240 offset:4608\n"
        "v_mfma_f32_16x16x32_bf16 v[220:223], v[192:195], v[112:115], v[220:223]\n"
        "ds_read_b64_tr_b16 v[188:189], v241 offset:4096\n"
        "v_mfma_f32_16x16x32_bf16 v[224:227], v[196:199], v[112:115], v[224:227]\n"
        "ds_read_b128 v[120:123], v242 offset:17408\n"
        "v_mfma_f32_16x16x32_bf16 v[228:231], v[192:195], v[208:211], v[228:231]\n"
        "ds_read_b64_tr_b16 v[190:191], v241 offset:4608\n"
        "v_mfma_f32_16x16x32_bf16 v[232:235], v[196:199], v[208:211], v[232:235]\n"
        "ds_read_b128 v[204:207], v246 offset:8192\n"
        "s_waitcnt vmcnt(0) lgkmcnt(7)\n"
        "s_barrier\n"
        "v_mfma_f32_16x16x32_bf16 v[212:215], v[176:179], v[44:47], v[212:215]\n"
        "ds_read_b64_tr_b16 v[192:193], v240 offset:8192\n"
        "ds_read_b128 v[52:55], v243 offset:20480\n"
        "v_mfma_f32_16x16x32_bf16 v[216:219], v[180:183], v[44:47], v[216:219]\n"
        "ds_read_b64_tr_b16 v[194:195], v240 offset:8704\n"
        "v_mfma_f32_16x16x32_bf16 v[220:223], v[176:179], v[116:119], v[220:223]\n"
        "ds_read_b64_tr_b16 v[196:197], v241 offset:8192\n"
        "v_mfma_f32_16x16x32_bf16 v[224:227], v[180:183], v[116:119], v[224:227]\n"
        "ds_read_b128 v[124:127], v243 offset:21504\n"
        "v_mfma_f32_16x16x32_bf16 v[228:231], v[176:179], v[200:203], v[228:231]\n"
        "ds_read_b64_tr_b16 v[198:199], v241 offset:8704\n"
        "v_mfma_f32_16x16x32_bf16 v[232:235], v[180:183], v[200:203], v[232:235]\n"
        "ds_read_b128 v[208:211], v247 offset:10240\n"
        "s_waitcnt lgkmcnt(7)\n"
        "v_mfma_f32_16x16x32_bf16 v[212:215], v[184:187], v[48:51], v[212:215]\n"
        "ds_read_b64_tr_b16 v[176:177], v240 offset:12288\n"
        "ds_read_b128 v[56:59], v242 offset:24576\n"
        "v_mfma_f32_16x16x32_bf16 v[216:219], v[188:191], v[48:51], v[216:219]\n"
        "ds_read_b64_tr_b16 v[178:179], v240 offset:12800\n"
        "v_mfma_f32_16x16x32_bf16 v[220:223], v[184:187], v[120:123], v[220:223]\n"
        "ds_read_b64_tr_b16 v[180:181], v241 offset:12288\n"
        "v_mfma_f32_16x16x32_bf16 v[224:227], v[188:191], v[120:123], v[224:227]\n"
        "ds_read_b128 v[128:131], v242 offset:25600\n"
        "v_mfma_f32_16x16x32_bf16 v[228:231], v[184:187], v[204:207], v[228:231]\n"
        "ds_read_b64_tr_b16 v[182:183], v241 offset:12800\n"
        "v_mfma_f32_16x16x32_bf16 v[232:235], v[188:191], v[204:207], v[232:235]\n"
        "ds_read_b128 v[200:203], v246 offset:12288\n"
        "s_waitcnt lgkmcnt(7)\n"
        "v_mfma_f32_16x16x32_bf16 v[212:215], v[192:195], v[52:55], v[212:215]\n"
        "ds_read_b64_tr_b16 v[184:185], v240 offset:16384\n"
        "ds_read_b128 v[60:63], v243 offset:28672\n"
        "v_mfma_f32_16x16x32_bf16 v[216:219], v[196:199], v[52:55], v[216:219]\n"
        "ds_read_b64_tr_b16 v[186:187], v240 offset:16896\n"
        "v_mfma_f32_16x16x32_bf16 v[220:223], v[192:195], v[124:127], v[220:223]\n"
        "ds_read_b64_tr_b16 v[188:189], v241 offset:16384\n"
        "v_mfma_f32_16x16x32_bf16 v[224:227], v[196:199], v[124:127], v[224:227]\n"
        "ds_read_b128 v[132:135], v243 offset:29696\n"
        "v_mfma_f32_16x16x32_bf16 v[228:231], v[192:195], v[208:211], v[228:231]\n"
        "ds_read_b64_tr_b16 v[190:191], v241 offset:16896\n"
        "v_mfma_f32_16x16x32_bf16 v[232:235], v[196:199], v[208:211], v[232:235]\n"
        "ds_read_b128 v[204:207], v247 offset:14336\n"
        "s_waitcnt lgkmcnt(7)\n"
        "v_mfma_f32_16x16x32_bf16 v[212:215], v[176:179], v[56:59], v[212:215]\n"
        "ds_read_b64_tr_b16 v[192:193], v240 offset:20480\n"
        "ds_read_b128 v[64:67], v242 offset:32768\n"
        "v_mfma_f32_16x16x32_bf16 v[216:219], v[180:183], v[56:59], v[216:219]\n"
        "ds_read_b64_tr_b16 v[194:195], v240 offset:20992\n"
        "v_mfma_f32_16x16x32_bf16 v[220:223], v[176:179], v[128:131], v[220:223]\n"
        "ds_read_b64_tr_b16 v[196:197], v241 offset:20480\n"
        "v_mfma_f32_16x16x32_bf16 v[224:227], v[180:183], v[128:131], v[224:227]\n"
        "ds_read_b128 v[136:139], v242 offset:33792\n"
        "v_mfma_f32_16x16x32_bf16 v[228:231], v[176:179], v[200:203], v[228:231]\n"
        "ds_read_b64_tr_b16 v[198:199], v241 offset:20992\n"
        "v_mfma_f32_16x16x32_bf16 v[232:235], v[180:183], v[200:203], v[232:235]\n"
        "ds_read_b128 v[208:211], v246 offset:16384\n"
        "s_waitcnt lgkmcnt(7)\n"
        "v_mfma_f32_16x16x32_bf16 v[212:215], v[184:187], v[60:63], v[212:215]\n"
        "ds_read_b64_tr_b16 v[176:177], v240 offset:24576\n"
        "ds_read_b128 v[68:71], v243 offset:36864\n"
        "v_mfma_f32_16x16x32_bf16 v[216:219], v[188:191], v[60:63], v[216:219]\n"
        "ds_read_b64_tr_b16 v[178:179], v240 offset:25088\n"
        "v_mfma_f32_16x16x32_bf16 v[220:223], v[184:187], v[132:135], v[220:223]\n"
        "ds_read_b64_tr_b16 v[180:181], v241 offset:24576\n"
        "v_mfma_f32_16x16x32_bf16 v[224:227], v[188:191], v[132:135], v[224:227]\n"
        "ds_read_b128 v[140:143], v243 offset:37888\n"
        "v_mfma_f32_16x16x32_bf16 v[228:231], v[184:187], v[204:207], v[228:231]\n"
        "ds_read_b64_tr_b16 v[182:183], v241 offset:25088\n"
        "v_mfma_f32_16x16x32_bf16 v[232:235], v[188:191], v[204:207], v[232:235]\n"
        "ds_read_b128 v[200:203], v247 offset:18432\n"
        "s_waitcnt lgkmcnt(7)\n"
        "s_barrier\n"
        "v_mfma_f32_16x16x32_bf16 v[212:215], v[192:195], v[64:67], v[212:215]\n"
        "ds_read_b64_tr_b16 v[184:185], v240 offset:28672\n"
        "ds_read_b128 v[72:75], v242 offset:40960\n"
        "v_mfma_f32_16x16x32_bf16 v[216:219], v[196:199], v[64:67], v[216:219]\n"
        "ds_read_b64_tr_b16 v[186:187], v240 offset:29184\n"
        "v_mfma_f32_16x16x32_bf16 v[220:223], v[192:195], v[136:139], v[220:223]\n"
        "ds_read_b64_tr_b16 v[188:189], v241 offset:28672\n"
        "v_mfma_f32_16x16x32_bf16 v[224:227], v[196:199], v[136:139], v[224:227]\n"
        "ds_read_b128 v[144:147], v242 offset:41984\n"
        "v_mfma_f32_16x16x32_bf16 v[228:231], v[192:195], v[208:211], v[228:231]\n"
        "ds_read_b64_tr_b16 v[190:191], v241 offset:29184\n"
        "v_mfma_f32_16x16x32_bf16 v[232:235], v[196:199], v[208:211], v[232:235]\n"
        "ds_read_b128 v[204:207], v246 offset:20480\n"
        "s_waitcnt lgkmcnt(7)\n"
        "v_mfma_f32_16x16x32_bf16 v[212:215], v[176:179], v[68:71], v[212:215]\n"
        "ds_read_b64_tr_b16 v[192:193], v238 offset:49152\n"
        "ds_read_b128 v[76:79], v243 offset:45056\n"
        "v_mfma_f32_16x16x32_bf16 v[216:219], v[180:183], v[68:71], v[216:219]\n"
        "ds_read_b64_tr_b16 v[194:195], v238 offset:49664\n"
        "v_mfma_f32_16x16x32_bf16 v[220:223], v[176:179], v[140:143], v[220:223]\n"
        "ds_read_b64_tr_b16 v[196:197], v239 offset:49152\n"
        "v_mfma_f32_16x16x32_bf16 v[224:227], v[180:183], v[140:143], v[224:227]\n"
        "ds_read_b128 v[148:151], v243 offset:46080\n"
        "v_mfma_f32_16x16x32_bf16 v[228:231], v[176:179], v[200:203], v[228:231]\n"
        "ds_read_b64_tr_b16 v[198:199], v239 offset:49664\n"
        "v_mfma_f32_16x16x32_bf16 v[232:235], v[180:183], v[200:203], v[232:235]\n"
        "ds_read_b128 v[208:211], v247 offset:22528\n"
        "s_waitcnt lgkmcnt(7)\n"
        "v_mfma_f32_16x16x32_bf16 v[212:215], v[184:187], v[72:75], v[212:215]\n"
        "ds_read_b64_tr_b16 v[176:177], v238 offset:45056\n"
        "ds_read_b128 v[80:83], v242 offset:49152\n"
        "v_mfma_f32_16x16x32_bf16 v[216:219], v[188:191], v[72:75], v[216:219]\n"
        "ds_read_b64_tr_b16 v[178:179], v238 offset:45568\n"
        "v_mfma_f32_16x16x32_bf16 v[220:223], v[184:187], v[144:147], v[220:223]\n"
        "ds_read_b64_tr_b16 v[180:181], v239 offset:45056\n"
        "v_mfma_f32_16x16x32_bf16 v[224:227], v[188:191], v[144:147], v[224:227]\n"
        "ds_read_b128 v[152:155], v242 offset:50176\n"
        "v_mfma_f32_16x16x32_bf16 v[228:231], v[184:187], v[204:207], v[228:231]\n"
        "ds_read_b64_tr_b16 v[182:183], v239 offset:45568\n"
        "v_mfma_f32_16x16x32_bf16 v[232:235], v[188:191], v[204:207], v[232:235]\n"
        "ds_read_b128 v[200:203], v246 offset:24576\n"
        "s_waitcnt lgkmcnt(7)\n"
        "v_mfma_f32_16x16x32_bf16 v[212:215], v[192:195], v[76:79], v[212:215]\n"
        "ds_read_b64_tr_b16 v[184:185], v240 offset:0\n"
        "ds_read_b128 v[84:87], v243 offset:53248\n"
        "v_mfma_f32_16x16x32_bf16 v[216:219], v[196:199], v[76:79], v[216:219]\n"
        "ds_read_b64_tr_b16 v[186:187], v240 offset:512\n"
        "v_mfma_f32_16x16x32_bf16 v[220:223], v[192:195], v[148:151], v[220:223]\n"
        "ds_read_b64_tr_b16 v[188:189], v241 offset:0\n"
        "v_mfma_f32_16x16x32_bf16 v[224:227], v[196:199], v[148:151], v[224:227]\n"
        "ds_read_b128 v[156:159], v243 offset:54272\n"
        "v_mfma_f32_16x16x32_bf16 v[228:231], v[192:195], v[208:211], v[228:231]\n"
        "ds_read_b64_tr_b16 v[190:191], v241 offset:512\n"
        "v_mfma_f32_16x16x32_bf16 v[232:235], v[196:199], v[208:211], v[232:235]\n"
        "ds_read_b128 v[204:207], v247 offset:26624\n"
        "s_waitcnt lgkmcnt(7)\n"
        "v_mfma_f32_16x16x32_bf16 v[212:215], v[176:179], v[80:83], v[212:215]\n"
        "ds_read_b64_tr_b16 v[192:193], v238 offset:61440\n"
        "ds_read_b128 v[88:91], v242 offset:57344\n"
        "v_mfma_f32_16x16x32_bf16 v[216:219], v[180:183], v[80:83], v[216:219]\n"
        "ds_read_b64_tr_b16 v[194:195], v238 offset:61952\n"
        "v_mfma_f32_16x16x32_bf16 v[220:223], v[176:179], v[152:155], v[220:223]\n"
        "ds_read_b64_tr_b16 v[196:197], v239 offset:61440\n"
        "v_mfma_f32_16x16x32_bf16 v[224:227], v[180:183], v[152:155], v[224:227]\n"
        "ds_read_b128 v[160:163], v242 offset:58368\n"
        "v_mfma_f32_16x16x32_bf16 v[228:231], v[176:179], v[200:203], v[228:231]\n"
        "ds_read_b64_tr_b16 v[198:199], v239 offset:61952\n"
        "v_mfma_f32_16x16x32_bf16 v[232:235], v[180:183], v[200:203], v[232:235]\n"
        "ds_read_b128 v[208:211], v246 offset:28672\n"
        "s_waitcnt lgkmcnt(7)\n"
        "s_barrier\n"
        "v_mfma_f32_16x16x32_bf16 v[212:215], v[184:187], v[84:87], v[212:215]\n"
        "ds_read_b64_tr_b16 v[176:177], v238 offset:57344\n"
        "ds_read_b128 v[92:95], v243 offset:61440\n"
        "v_mfma_f32_16x16x32_bf16 v[216:219], v[188:191], v[84:87], v[216:219]\n"
        "ds_read_b64_tr_b16 v[178:179], v238 offset:57856\n"
        "v_mfma_f32_16x16x32_bf16 v[220:223], v[184:187], v[156:159], v[220:223]\n"
        "ds_read_b64_tr_b16 v[180:181], v239 offset:57344\n"
        "v_mfma_f32_16x16x32_bf16 v[224:227], v[188:191], v[156:159], v[224:227]\n"
        "ds_read_b128 v[164:167], v243 offset:62464\n"
        "v_mfma_f32_16x16x32_bf16 v[228:231], v[184:187], v[204:207], v[228:231]\n"
        "ds_read_b64_tr_b16 v[182:183], v239 offset:57856\n"
        "v_mfma_f32_16x16x32_bf16 v[232:235], v[188:191], v[204:207], v[232:235]\n"
        "ds_read_b128 v[200:203], v247 offset:30720\n"
        "s_waitcnt lgkmcnt(7)\n"
        "v_mfma_f32_16x16x32_bf16 v[212:215], v[192:195], v[88:91], v[212:215]\n"
        "ds_read_b64_tr_b16 v[184:185], v238 offset:53248\n"
        "ds_read_b128 v[96:99], v244 offset:0\n"
        "v_mfma_f32_16x16x32_bf16 v[216:219], v[196:199], v[88:91], v[216:219]\n"
        "ds_read_b64_tr_b16 v[186:187], v238 offset:53760\n"
        "v_mfma_f32_16x16x32_bf16 v[220:223], v[192:195], v[160:163], v[220:223]\n"
        "ds_read_b64_tr_b16 v[188:189], v239 offset:53248\n"
        "v_mfma_f32_16x16x32_bf16 v[224:227], v[196:199], v[160:163], v[224:227]\n"
        "ds_read_b128 v[168:171], v244 offset:1024\n"
        "v_mfma_f32_16x16x32_bf16 v[228:231], v[192:195], v[208:211], v[228:231]\n"
        "ds_read_b64_tr_b16 v[190:191], v239 offset:53760\n"
        "v_mfma_f32_16x16x32_bf16 v[232:235], v[196:199], v[208:211], v[232:235]\n"
        "ds_read_b128 v[204:207], v246 offset:32768\n"
        "s_waitcnt lgkmcnt(7)\n"
        "v_mfma_f32_16x16x32_bf16 v[212:215], v[176:179], v[92:95], v[212:215]\n"
        "ds_read_b64_tr_b16 v[192:193], v236 offset:12288\n"
        "ds_read_b128 v[100:103], v245 offset:4096\n"
        "v_mfma_f32_16x16x32_bf16 v[216:219], v[180:183], v[92:95], v[216:219]\n"
        "ds_read_b64_tr_b16 v[194:195], v236 offset:12800\n"
        "v_mfma_f32_16x16x32_bf16 v[220:223], v[176:179], v[164:167], v[220:223]\n"
        "ds_read_b64_tr_b16 v[196:197], v237 offset:12288\n"
        "v_mfma_f32_16x16x32_bf16 v[224:227], v[180:183], v[164:167], v[224:227]\n"
        "ds_read_b128 v[172:175], v245 offset:5120\n"
        "v_mfma_f32_16x16x32_bf16 v[228:231], v[176:179], v[200:203], v[228:231]\n"
        "ds_read_b64_tr_b16 v[198:199], v237 offset:12800\n"
        "v_mfma_f32_16x16x32_bf16 v[232:235], v[180:183], v[200:203], v[232:235]\n"
        "ds_read_b128 v[208:211], v247 offset:34816\n"
        "s_waitcnt lgkmcnt(7)\n"
        "v_mfma_f32_16x16x32_bf16 v[212:215], v[184:187], v[96:99], v[212:215]\n"
        "ds_read_b64_tr_b16 v[176:177], v236 offset:8192\n"
        "v_mfma_f32_16x16x32_bf16 v[216:219], v[188:191], v[96:99], v[216:219]\n"
        "ds_read_b64_tr_b16 v[178:179], v236 offset:8704\n"
        "v_mfma_f32_16x16x32_bf16 v[220:223], v[184:187], v[168:171], v[220:223]\n"
        "ds_read_b64_tr_b16 v[180:181], v237 offset:8192\n"
        "v_mfma_f32_16x16x32_bf16 v[224:227], v[188:191], v[168:171], v[224:227]\n"
        "ds_read_b64_tr_b16 v[182:183], v237 offset:8704\n"
        "v_mfma_f32_16x16x32_bf16 v[228:231], v[184:187], v[204:207], v[228:231]\n"
        "ds_read_b128 v[200:203], v246 offset:0\n"
        "v_mfma_f32_16x16x32_bf16 v[232:235], v[188:191], v[204:207], v[232:235]\n"
        "s_waitcnt lgkmcnt(5)\n"
        "v_mfma_f32_16x16x32_bf16 v[212:215], v[192:195], v[100:103], v[212:215]\n"
        "ds_read_b64_tr_b16 v[184:185], v236 offset:4096\n"
        "v_mfma_f32_16x16x32_bf16 v[216:219], v[196:199], v[100:103], v[216:219]\n"
        "ds_read_b64_tr_b16 v[186:187], v236 offset:4608\n"
        "v_mfma_f32_16x16x32_bf16 v[220:223], v[192:195], v[172:175], v[220:223]\n"
        "ds_read_b64_tr_b16 v[188:189], v237 offset:4096\n"
        "v_mfma_f32_16x16x32_bf16 v[224:227], v[196:199], v[172:175], v[224:227]\n"
        "ds_read_b64_tr_b16 v[190:191], v237 offset:4608\n"
        "v_mfma_f32_16x16x32_bf16 v[228:231], v[192:195], v[208:211], v[228:231]\n"
        "ds_read_b128 v[204:207], v247 offset:2048\n"
        "v_mfma_f32_16x16x32_bf16 v[232:235], v[196:199], v[208:211], v[232:235]\n"
        "s_waitcnt lgkmcnt(5)\n"
        "s_barrier\n"
        "v_cvt_pk_bf16_f32 v6, v212, v213\n"
        "v_cvt_pk_bf16_f32 v7, v214, v215\n"
        "v_mfma_f32_16x16x32_bf16 v[212:215], v[176:179], v[32:35], 0\n"
        "ds_read_b64_tr_b16 v[192:193], v236 offset:0\n"
        "v_cvt_pk_bf16_f32 v8, v216, v217\n"
        "v_cvt_pk_bf16_f32 v9, v218, v219\n"
        "v_mfma_f32_16x16x32_bf16 v[216:219], v[180:183], v[32:35], 0\n"
        "ds_read_b64_tr_b16 v[194:195], v236 offset:512\n"
        "v_cvt_pk_bf16_f32 v10, v220, v221\n"
        "v_cvt_pk_bf16_f32 v11, v222, v223\n"
        "v_mfma_f32_16x16x32_bf16 v[220:223], v[176:179], v[104:107], 0\n"
        "ds_read_b64_tr_b16 v[196:197], v237 offset:0\n"
        "v_cvt_pk_bf16_f32 v12, v224, v225\n"
        "v_cvt_pk_bf16_f32 v13, v226, v227\n"
        "v_mfma_f32_16x16x32_bf16 v[224:227], v[180:183], v[104:107], 0\n"
        "ds_read_b64_tr_b16 v[198:199], v237 offset:512\n"
        "v_cvt_pk_bf16_f32 v14, v228, v229\n"
        "v_cvt_pk_bf16_f32 v15, v230, v231\n"
        "v_mfma_f32_16x16x32_bf16 v[228:231], v[176:179], v[200:203], 0\n"
        "ds_read_b128 v[208:211], v246 offset:4096\n"
        "v_cvt_pk_bf16_f32 v16, v232, v233\n"
        "v_cvt_pk_bf16_f32 v17, v234, v235\n"
        "v_mfma_f32_16x16x32_bf16 v[232:235], v[180:183], v[200:203], 0\n"
        "s_waitcnt lgkmcnt(5)\n"
        "v_mfma_f32_16x16x32_bf16 v[212:215], v[184:187], v[36:39], v[212:215]\n"
        "ds_read_b64_tr_b16 v[176:177], v240 offset:20480\n"
        "v_mfma_f32_16x16x32_bf16 v[216:219], v[188:191], v[36:39], v[216:219]\n"
        "ds_read_b64_tr_b16 v[178:179], v240 offset:20992\n"
        "v_mfma_f32_16x16x32_bf16 v[220:223], v[184:187], v[108:111], v[220:223]\n"
        "ds_read_b64_tr_b16 v[180:181], v241 offset:20480\n"
        "v_mfma_f32_16x16x32_bf16 v[224:227], v[188:191], v[108:111], v[224:227]\n"
        "ds_read_b64_tr_b16 v[182:183], v241 offset:20992\n"
        "v_mfma_f32_16x16x32_bf16 v[228:231], v[184:187], v[204:207], v[228:231]\n"
        "ds_read_b128 v[200:203], v247 offset:6144\n"
        "v_mfma_f32_16x16x32_bf16 v[232:235], v[188:191], v[204:207], v[232:235]\n"
        "s_waitcnt lgkmcnt(5)\n"
        "v_mfma_f32_16x16x32_bf16 v[212:215], v[192:195], v[40:43], v[212:215]\n"
        "ds_read_b64_tr_b16 v[184:185], v240 offset:16384\n"
        "v_mfma_f32_16x16x32_bf16 v[216:219], v[196:199], v[40:43], v[216:219]\n"
        "ds_read_b64_tr_b16 v[186:187], v240 offset:16896\n"
        "v_mfma_f32_16x16x32_bf16 v[220:223], v[192:195], v[112:115], v[220:223]\n"
        "ds_read_b64_tr_b16 v[188:189], v241 offset:16384\n"
        "v_mfma_f32_16x16x32_bf16 v[224:227], v[196:199], v[112:115], v[224:227]\n"
        "ds_read_b64_tr_b16 v[190:191], v241 offset:16896\n"
        "v_mfma_f32_16x16x32_bf16 v[228:231], v[192:195], v[208:211], v[228:231]\n"
        "ds_read_b128 v[204:207], v246 offset:8192\n"
        "v_mfma_f32_16x16x32_bf16 v[232:235], v[196:199], v[208:211], v[232:235]\n"
        "s_waitcnt lgkmcnt(5)\n"
        "v_mfma_f32_16x16x32_bf16 v[212:215], v[176:179], v[44:47], v[212:215]\n"
        "ds_read_b64_tr_b16 v[192:193], v240 offset:12288\n"
        "v_mfma_f32_16x16x32_bf16 v[216:219], v[180:183], v[44:47], v[216:219]\n"
        "ds_read_b64_tr_b16 v[194:195], v240 offset:12800\n"
        "v_mfma_f32_16x16x32_bf16 v[220:223], v[176:179], v[116:119], v[220:223]\n"
        "ds_read_b64_tr_b16 v[196:197], v241 offset:12288\n"
        "v_mfma_f32_16x16x32_bf16 v[224:227], v[180:183], v[116:119], v[224:227]\n"
        "ds_read_b64_tr_b16 v[198:199], v241 offset:12800\n"
        "v_mfma_f32_16x16x32_bf16 v[228:231], v[176:179], v[200:203], v[228:231]\n"
        "ds_read_b128 v[208:211], v247 offset:10240\n"
        "v_mfma_f32_16x16x32_bf16 v[232:235], v[180:183], v[200:203], v[232:235]\n"
        "s_waitcnt lgkmcnt(5)\n"
        "v_mfma_f32_16x16x32_bf16 v[212:215], v[184:187], v[48:51], v[212:215]\n"
        "ds_read_b64_tr_b16 v[176:177], v240 offset:8192\n"
        "v_mfma_f32_16x16x32_bf16 v[216:219], v[188:191], v[48:51], v[216:219]\n"
        "ds_read_b64_tr_b16 v[178:179], v240 offset:8704\n"
        "v_mfma_f32_16x16x32_bf16 v[220:223], v[184:187], v[120:123], v[220:223]\n"
        "ds_read_b64_tr_b16 v[180:181], v241 offset:8192\n"
        "v_mfma_f32_16x16x32_bf16 v[224:227], v[188:191], v[120:123], v[224:227]\n"
        "ds_read_b64_tr_b16 v[182:183], v241 offset:8704\n"
        "v_mfma_f32_16x16x32_bf16 v[228:231], v[184:187], v[204:207], v[228:231]\n"
        "ds_read_b128 v[200:203], v246 offset:12288\n"
        "v_mfma_f32_16x16x32_bf16 v[232:235], v[188:191], v[204:207], v[232:235]\n"
        "s_waitcnt lgkmcnt(5)\n"
        "v_mfma_f32_16x16x32_bf16 v[212:215], v[192:195], v[52:55], v[212:215]\n"
        "ds_read_b64_tr_b16 v[184:185], v240 offset:4096\n"
        "v_mfma_f32_16x16x32_bf16 v[216:219], v[196:199], v[52:55], v[216:219]\n"
        "ds_read_b64_tr_b16 v[186:187], v240 offset:4608\n"
        "v_mfma_f32_16x16x32_bf16 v[220:223], v[192:195], v[124:127], v[220:223]\n"
        "ds_read_b64_tr_b16 v[188:189], v241 offset:4096\n"
        "v_mfma_f32_16x16x32_bf16 v[224:227], v[196:199], v[124:127], v[224:227]\n"
        "ds_read_b64_tr_b16 v[190:191], v241 offset:4608\n"
        "v_mfma_f32_16x16x32_bf16 v[228:231], v[192:195], v[208:211], v[228:231]\n"
        "ds_read_b128 v[204:207], v247 offset:14336\n"
        "v_mfma_f32_16x16x32_bf16 v[232:235], v[196:199], v[208:211], v[232:235]\n"
        "s_waitcnt lgkmcnt(5)\n"
        "s_barrier\n"
        "v_mfma_f32_16x16x32_bf16 v[212:215], v[176:179], v[56:59], v[212:215]\n"
        "ds_read_b64_tr_b16 v[192:193], v236 offset:32768\n"
        "v_mfma_f32_16x16x32_bf16 v[216:219], v[180:183], v[56:59], v[216:219]\n"
        "ds_read_b64_tr_b16 v[194:195], v236 offset:33280\n"
        "v_mfma_f32_16x16x32_bf16 v[220:223], v[176:179], v[128:131], v[220:223]\n"
        "ds_read_b64_tr_b16 v[196:197], v237 offset:32768\n"
        "v_mfma_f32_16x16x32_bf16 v[224:227], v[180:183], v[128:131], v[224:227]\n"
        "ds_read_b64_tr_b16 v[198:199], v237 offset:33280\n"
        "v_mfma_f32_16x16x32_bf16 v[228:231], v[176:179], v[200:203], v[228:231]\n"
        "ds_read_b128 v[208:211], v246 offset:16384\n"
        "v_mfma_f32_16x16x32_bf16 v[232:235], v[180:183], v[200:203], v[232:235]\n"
        "s_waitcnt lgkmcnt(5)\n"
        "v_mfma_f32_16x16x32_bf16 v[212:215], v[184:187], v[60:63], v[212:215]\n"
        "ds_read_b64_tr_b16 v[176:177], v236 offset:28672\n"
        "v_mfma_f32_16x16x32_bf16 v[216:219], v[188:191], v[60:63], v[216:219]\n"
        "ds_read_b64_tr_b16 v[178:179], v236 offset:29184\n"
        "v_mfma_f32_16x16x32_bf16 v[220:223], v[184:187], v[132:135], v[220:223]\n"
        "ds_read_b64_tr_b16 v[180:181], v237 offset:28672\n"
        "v_mfma_f32_16x16x32_bf16 v[224:227], v[188:191], v[132:135], v[224:227]\n"
        "ds_read_b64_tr_b16 v[182:183], v237 offset:29184\n"
        "v_mfma_f32_16x16x32_bf16 v[228:231], v[184:187], v[204:207], v[228:231]\n"
        "ds_read_b128 v[200:203], v247 offset:18432\n"
        "v_mfma_f32_16x16x32_bf16 v[232:235], v[188:191], v[204:207], v[232:235]\n"
        "s_waitcnt lgkmcnt(5)\n"
        "v_mfma_f32_16x16x32_bf16 v[212:215], v[192:195], v[64:67], v[212:215]\n"
        "ds_read_b64_tr_b16 v[184:185], v236 offset:24576\n"
        "v_mfma_f32_16x16x32_bf16 v[216:219], v[196:199], v[64:67], v[216:219]\n"
        "ds_read_b64_tr_b16 v[186:187], v236 offset:25088\n"
        "v_mfma_f32_16x16x32_bf16 v[220:223], v[192:195], v[136:139], v[220:223]\n"
        "ds_read_b64_tr_b16 v[188:189], v237 offset:24576\n"
        "v_mfma_f32_16x16x32_bf16 v[224:227], v[196:199], v[136:139], v[224:227]\n"
        "ds_read_b64_tr_b16 v[190:191], v237 offset:25088\n"
        "v_mfma_f32_16x16x32_bf16 v[228:231], v[192:195], v[208:211], v[228:231]\n"
        "ds_read_b128 v[204:207], v246 offset:20480\n"
        "v_mfma_f32_16x16x32_bf16 v[232:235], v[196:199], v[208:211], v[232:235]\n"
        "s_waitcnt lgkmcnt(5)\n"
        "v_mfma_f32_16x16x32_bf16 v[212:215], v[176:179], v[68:71], v[212:215]\n"
        "ds_read_b64_tr_b16 v[192:193], v236 offset:20480\n"
        "v_mfma_f32_16x16x32_bf16 v[216:219], v[180:183], v[68:71], v[216:219]\n"
        "ds_read_b64_tr_b16 v[194:195], v236 offset:20992\n"
        "v_mfma_f32_16x16x32_bf16 v[220:223], v[176:179], v[140:143], v[220:223]\n"
        "ds_read_b64_tr_b16 v[196:197], v237 offset:20480\n"
        "v_mfma_f32_16x16x32_bf16 v[224:227], v[180:183], v[140:143], v[224:227]\n"
        "ds_read_b64_tr_b16 v[198:199], v237 offset:20992\n"
        "v_mfma_f32_16x16x32_bf16 v[228:231], v[176:179], v[200:203], v[228:231]\n"
        "ds_read_b128 v[208:211], v247 offset:22528\n"
        "v_mfma_f32_16x16x32_bf16 v[232:235], v[180:183], v[200:203], v[232:235]\n"
        "s_waitcnt lgkmcnt(5)\n"
        "v_mfma_f32_16x16x32_bf16 v[212:215], v[184:187], v[72:75], v[212:215]\n"
        "ds_read_b64_tr_b16 v[176:177], v240 offset:28672\n"
        "v_mfma_f32_16x16x32_bf16 v[216:219], v[188:191], v[72:75], v[216:219]\n"
        "ds_read_b64_tr_b16 v[178:179], v240 offset:29184\n"
        "v_mfma_f32_16x16x32_bf16 v[220:223], v[184:187], v[144:147], v[220:223]\n"
        "ds_read_b64_tr_b16 v[180:181], v241 offset:28672\n"
        "v_mfma_f32_16x16x32_bf16 v[224:227], v[188:191], v[144:147], v[224:227]\n"
        "ds_read_b64_tr_b16 v[182:183], v241 offset:29184\n"
        "v_mfma_f32_16x16x32_bf16 v[228:231], v[184:187], v[204:207], v[228:231]\n"
        "ds_read_b128 v[200:203], v246 offset:24576\n"
        "v_mfma_f32_16x16x32_bf16 v[232:235], v[188:191], v[204:207], v[232:235]\n"
        "s_waitcnt lgkmcnt(5)\n"
        "v_mfma_f32_16x16x32_bf16 v[212:215], v[192:195], v[76:79], v[212:215]\n"
        "ds_read_b64_tr_b16 v[184:185], v240 offset:24576\n"
        "v_mfma_f32_16x16x32_bf16 v[216:219], v[196:199], v[76:79], v[216:219]\n"
        "ds_read_b64_tr_b16 v[186:187], v240 offset:25088\n"
        "v_mfma_f32_16x16x32_bf16 v[220:223], v[192:195], v[148:151], v[220:223]\n"
        "ds_read_b64_tr_b16 v[188:189], v241 offset:24576\n"
        "v_mfma_f32_16x16x32_bf16 v[224:227], v[196:199], v[148:151], v[224:227]\n"
        "ds_read_b64_tr_b16 v[190:191], v241 offset:25088\n"
        "v_mfma_f32_16x16x32_bf16 v[228:231], v[192:195], v[208:211], v[228:231]\n"
        "ds_read_b128 v[204:207], v247 offset:26624\n"
        "v_mfma_f32_16x16x32_bf16 v[232:235], v[196:199], v[208:211], v[232:235]\n"
        "s_waitcnt lgkmcnt(5)\n"
        "s_barrier\n"
        "v_mfma_f32_16x16x32_bf16 v[212:215], v[176:179], v[80:83], v[212:215]\n"
        "ds_read_b64_tr_b16 v[192:193], v240 offset:0\n"
        "v_mfma_f32_16x16x32_bf16 v[216:219], v[180:183], v[80:83], v[216:219]\n"
        "ds_read_b64_tr_b16 v[194:195], v240 offset:512\n"
        "v_mfma_f32_16x16x32_bf16 v[220:223], v[176:179], v[152:155], v[220:223]\n"
        "ds_read_b64_tr_b16 v[196:197], v241 offset:0\n"
        "v_mfma_f32_16x16x32_bf16 v[224:227], v[180:183], v[152:155], v[224:227]\n"
        "ds_read_b64_tr_b16 v[198:199], v241 offset:512\n"
        "v_mfma_f32_16x16x32_bf16 v[228:231], v[176:179], v[200:203], v[228:231]\n"
        "ds_read_b128 v[208:211], v246 offset:28672\n"
        "v_mfma_f32_16x16x32_bf16 v[232:235], v[180:183], v[200:203], v[232:235]\n"
        "s_waitcnt lgkmcnt(5)\n"
        "v_mfma_f32_16x16x32_bf16 v[212:215], v[184:187], v[84:87], v[212:215]\n"
        "ds_read_b64_tr_b16 v[176:177], v238 offset:49152\n"
        "v_mfma_f32_16x16x32_bf16 v[216:219], v[188:191], v[84:87], v[216:219]\n"
        "ds_read_b64_tr_b16 v[178:179], v238 offset:49664\n"
        "v_mfma_f32_16x16x32_bf16 v[220:223], v[184:187], v[156:159], v[220:223]\n"
        "ds_read_b64_tr_b16 v[180:181], v239 offset:49152\n"
        "v_mfma_f32_16x16x32_bf16 v[224:227], v[188:191], v[156:159], v[224:227]\n"
        "ds_read_b64_tr_b16 v[182:183], v239 offset:49664\n"
        "v_mfma_f32_16x16x32_bf16 v[228:231], v[184:187], v[204:207], v[228:231]\n"
        "ds_read_b128 v[200:203], v247 offset:30720\n"
        "v_mfma_f32_16x16x32_bf16 v[232:235], v[188:191], v[204:207], v[232:235]\n"
        "s_waitcnt lgkmcnt(5)\n"
        "v_mfma_f32_16x16x32_bf16 v[212:215], v[192:195], v[88:91], v[212:215]\n"
        "ds_read_b64_tr_b16 v[184:185], v238 offset:45056\n"
        "v_mfma_f32_16x16x32_bf16 v[216:219], v[196:199], v[88:91], v[216:219]\n"
        "ds_read_b64_tr_b16 v[186:187], v238 offset:45568\n"
        "v_mfma_f32_16x16x32_bf16 v[220:223], v[192:195], v[160:163], v[220:223]\n"
        "ds_read_b64_tr_b16 v[188:189], v239 offset:45056\n"
        "v_mfma_f32_16x16x32_bf16 v[224:227], v[196:199], v[160:163], v[224:227]\n"
        "ds_read_b64_tr_b16 v[190:191], v239 offset:45568\n"
        "v_mfma_f32_16x16x32_bf16 v[228:231], v[192:195], v[208:211], v[228:231]\n"
        "ds_read_b128 v[204:207], v246 offset:32768\n"
        "v_mfma_f32_16x16x32_bf16 v[232:235], v[196:199], v[208:211], v[232:235]\n"
        "s_waitcnt lgkmcnt(5)\n"
        "v_mfma_f32_16x16x32_bf16 v[212:215], v[176:179], v[92:95], v[212:215]\n"
        "ds_write_b64 v30, v[6:7] offset:0\n"
        "ds_write_b64 v31, v[8:9] offset:0\n"
        "v_mfma_f32_16x16x32_bf16 v[216:219], v[180:183], v[92:95], v[216:219]\n"
        "ds_write_b64 v30, v[10:11] offset:4096\n"
        "v_mfma_f32_16x16x32_bf16 v[220:223], v[176:179], v[164:167], v[220:223]\n"
        "ds_read_b64_tr_b16 v[192:193], v236 offset:53248\n"
        "v_mfma_f32_16x16x32_bf16 v[224:227], v[180:183], v[164:167], v[224:227]\n"
        "ds_read_b64_tr_b16 v[194:195], v236 offset:53760\n"
        "ds_read_b64_tr_b16 v[196:197], v237 offset:53248\n"
        "v_mfma_f32_16x16x32_bf16 v[228:231], v[176:179], v[200:203], v[228:231]\n"
        "ds_read_b64_tr_b16 v[198:199], v237 offset:53760\n"
        "v_mfma_f32_16x16x32_bf16 v[232:235], v[180:183], v[200:203], v[232:235]\n"
        "ds_read_b128 v[208:211], v247 offset:34816\n"
        "s_waitcnt lgkmcnt(8)\n"
        "v_mfma_f32_16x16x32_bf16 v[212:215], v[184:187], v[96:99], v[212:215]\n"
        "ds_write_b64 v31, v[12:13] offset:4096\n"
        "ds_write_b64 v30, v[14:15] offset:20480\n"
        "v_mfma_f32_16x16x32_bf16 v[216:219], v[188:191], v[96:99], v[216:219]\n"
        "ds_write_b64 v31, v[16:17] offset:20480\n"
        "v_mfma_f32_16x16x32_bf16 v[220:223], v[184:187], v[168:171], v[220:223]\n"
        "ds_read_b64_tr_b16 v[176:177], v236 offset:49152\n"
        "v_mfma_f32_16x16x32_bf16 v[224:227], v[188:191], v[168:171], v[224:227]\n"
        "ds_read_b64_tr_b16 v[178:179], v236 offset:49664\n"
        "ds_read_b64_tr_b16 v[180:181], v237 offset:49152\n"
        "v_mfma_f32_16x16x32_bf16 v[228:231], v[184:187], v[204:207], v[228:231]\n"
        "ds_read_b64_tr_b16 v[182:183], v237 offset:49664\n"
        "v_mfma_f32_16x16x32_bf16 v[232:235], v[188:191], v[204:207], v[232:235]\n"
        "ds_read_b128 v[200:203], v246 offset:0\n"
        "s_waitcnt lgkmcnt(8)\n"
        "v_mfma_f32_16x16x32_bf16 v[212:215], v[192:195], v[100:103], v[212:215]\n"
        "ds_read_b64_tr_b16 v[184:185], v236 offset:45056\n"
        "v_mfma_f32_16x16x32_bf16 v[216:219], v[196:199], v[100:103], v[216:219]\n"
        "ds_read_b64_tr_b16 v[186:187], v236 offset:45568\n"
        "v_mfma_f32_16x16x32_bf16 v[220:223], v[192:195], v[172:175], v[220:223]\n"
        "ds_read_b64_tr_b16 v[188:189], v237 offset:45056\n"
        "v_mfma_f32_16x16x32_bf16 v[224:227], v[196:199], v[172:175], v[224:227]\n"
        "ds_read_b64_tr_b16 v[190:191], v237 offset:45568\n"
        "v_mfma_f32_16x16x32_bf16 v[228:231], v[192:195], v[208:211], v[228:231]\n"
        "ds_read_b128 v[204:207], v247 offset:2048\n"
        "v_mfma_f32_16x16x32_bf16 v[232:235], v[196:199], v[208:211], v[232:235]\n"
        "s_waitcnt lgkmcnt(5)\n"
        "s_barrier\n"
        "v_cvt_pk_bf16_f32 v18, v212, v213\n"
        "v_cvt_pk_bf16_f32 v19, v214, v215\n"
        "v_mfma_f32_16x16x32_bf16 v[212:215], v[176:179], v[32:35], 0\n"
        "ds_read_b64_tr_b16 v[192:193], v236 offset:40960\n"
        "v_cvt_pk_bf16_f32 v20, v216, v217\n"
        "v_cvt_pk_bf16_f32 v21, v218, v219\n"
        "v_mfma_f32_16x16x32_bf16 v[216:219], v[180:183], v[32:35], 0\n"
        "ds_read_b64_tr_b16 v[194:195], v236 offset:41472\n"
        "v_cvt_pk_bf16_f32 v22, v220, v221\n"
        "v_cvt_pk_bf16_f32 v23, v222, v223\n"
        "v_mfma_f32_16x16x32_bf16 v[220:223], v[176:179], v[104:107], 0\n"
        "ds_read_b64_tr_b16 v[196:197], v237 offset:40960\n"
        "v_cvt_pk_bf16_f32 v24, v224, v225\n"
        "v_cvt_pk_bf16_f32 v25, v226, v227\n"
        "v_mfma_f32_16x16x32_bf16 v[224:227], v[180:183], v[104:107], 0\n"
        "ds_read_b64_tr_b16 v[198:199], v237 offset:41472\n"
        "v_cvt_pk_bf16_f32 v26, v228, v229\n"
        "v_cvt_pk_bf16_f32 v27, v230, v231\n"
        "v_mfma_f32_16x16x32_bf16 v[228:231], v[176:179], v[200:203], 0\n"
        "ds_read_b128 v[208:211], v246 offset:4096\n"
        "v_cvt_pk_bf16_f32 v28, v232, v233\n"
        "v_cvt_pk_bf16_f32 v29, v234, v235\n"
        "v_mfma_f32_16x16x32_bf16 v[232:235], v[180:183], v[200:203], 0\n"
        "s_waitcnt lgkmcnt(5)\n"
        "v_mfma_f32_16x16x32_bf16 v[212:215], v[184:187], v[36:39], v[212:215]\n"
        "ds_write_b64 v30, v[18:19] offset:24576\n"
        "ds_write_b64 v31, v[20:21] offset:24576\n"
        "v_mfma_f32_16x16x32_bf16 v[216:219], v[188:191], v[36:39], v[216:219]\n"
        "ds_write_b64 v30, v[22:23] offset:28672\n"
        "v_mfma_f32_16x16x32_bf16 v[220:223], v[184:187], v[108:111], v[220:223]\n"
        "ds_read_b64_tr_b16 v[176:177], v238 offset:61440\n"
        "v_mfma_f32_16x16x32_bf16 v[224:227], v[188:191], v[108:111], v[224:227]\n"
        "ds_read_b64_tr_b16 v[178:179], v238 offset:61952\n"
        "ds_read_b64_tr_b16 v[180:181], v239 offset:61440\n"
        "v_mfma_f32_16x16x32_bf16 v[228:231], v[184:187], v[204:207], v[228:231]\n"
        "ds_read_b64_tr_b16 v[182:183], v239 offset:61952\n"
        "v_mfma_f32_16x16x32_bf16 v[232:235], v[188:191], v[204:207], v[232:235]\n"
        "ds_read_b128 v[200:203], v247 offset:6144\n"
        "s_waitcnt lgkmcnt(8)\n"
        "v_mfma_f32_16x16x32_bf16 v[212:215], v[192:195], v[40:43], v[212:215]\n"
        "ds_write_b64 v31, v[24:25] offset:28672\n"
        "ds_write_b64 v30, v[26:27] offset:32768\n"
        "v_mfma_f32_16x16x32_bf16 v[216:219], v[196:199], v[40:43], v[216:219]\n"
        "ds_write_b64 v31, v[28:29] offset:32768\n"
        "v_mfma_f32_16x16x32_bf16 v[220:223], v[192:195], v[112:115], v[220:223]\n"
        "ds_read_b64_tr_b16 v[184:185], v238 offset:57344\n"
        "v_mfma_f32_16x16x32_bf16 v[224:227], v[196:199], v[112:115], v[224:227]\n"
        "ds_read_b64_tr_b16 v[186:187], v238 offset:57856\n"
        "ds_read_b64_tr_b16 v[188:189], v239 offset:57344\n"
        "v_mfma_f32_16x16x32_bf16 v[228:231], v[192:195], v[208:211], v[228:231]\n"
        "ds_read_b64_tr_b16 v[190:191], v239 offset:57856\n"
        "v_mfma_f32_16x16x32_bf16 v[232:235], v[196:199], v[208:211], v[232:235]\n"
        "ds_read_b128 v[204:207], v246 offset:8192\n"
        "s_waitcnt lgkmcnt(8)\n"
        "v_mfma_f32_16x16x32_bf16 v[212:215], v[176:179], v[44:47], v[212:215]\n"
        "ds_read_b64_tr_b16 v[192:193], v238 offset:53248\n"
        "v_mfma_f32_16x16x32_bf16 v[216:219], v[180:183], v[44:47], v[216:219]\n"
        "ds_read_b64_tr_b16 v[194:195], v238 offset:53760\n"
        "v_mfma_f32_16x16x32_bf16 v[220:223], v[176:179], v[116:119], v[220:223]\n"
        "ds_read_b64_tr_b16 v[196:197], v239 offset:53248\n"
        "v_mfma_f32_16x16x32_bf16 v[224:227], v[180:183], v[116:119], v[224:227]\n"
        "ds_read_b64_tr_b16 v[198:199], v239 offset:53760\n"
        "v_mfma_f32_16x16x32_bf16 v[228:231], v[176:179], v[200:203], v[228:231]\n"
        "ds_read_b128 v[208:211], v247 offset:10240\n"
        "v_mfma_f32_16x16x32_bf16 v[232:235], v[180:183], v[200:203], v[232:235]\n"
        "s_waitcnt lgkmcnt(5)\n"
        "s_barrier\n"
        "v_mfma_f32_16x16x32_bf16 v[212:215], v[184:187], v[48:51], v[212:215]\n"
        "ds_read_b64_tr_b16 v[176:177], v238 offset:4096\n"
        "v_mfma_f32_16x16x32_bf16 v[216:219], v[188:191], v[48:51], v[216:219]\n"
        "ds_read_b64_tr_b16 v[178:179], v238 offset:4608\n"
        "v_mfma_f32_16x16x32_bf16 v[220:223], v[184:187], v[120:123], v[220:223]\n"
        "ds_read_b64_tr_b16 v[180:181], v239 offset:4096\n"
        "v_mfma_f32_16x16x32_bf16 v[224:227], v[188:191], v[120:123], v[224:227]\n"
        "ds_read_b64_tr_b16 v[182:183], v239 offset:4608\n"
        "v_mfma_f32_16x16x32_bf16 v[228:231], v[184:187], v[204:207], v[228:231]\n"
        "ds_read_b128 v[200:203], v246 offset:12288\n"
        "v_mfma_f32_16x16x32_bf16 v[232:235], v[188:191], v[204:207], v[232:235]\n"
        "s_waitcnt lgkmcnt(5)\n"
        "v_mfma_f32_16x16x32_bf16 v[212:215], v[192:195], v[52:55], v[212:215]\n"
        "ds_read_b64_tr_b16 v[184:185], v238 offset:0\n"
        "v_mfma_f32_16x16x32_bf16 v[216:219], v[196:199], v[52:55], v[216:219]\n"
        "ds_read_b64_tr_b16 v[186:187], v238 offset:512\n"
        "v_mfma_f32_16x16x32_bf16 v[220:223], v[192:195], v[124:127], v[220:223]\n"
        "ds_read_b64_tr_b16 v[188:189], v239 offset:0\n"
        "v_mfma_f32_16x16x32_bf16 v[224:227], v[196:199], v[124:127], v[224:227]\n"
        "ds_read_b64_tr_b16 v[190:191], v239 offset:512\n"
        "v_mfma_f32_16x16x32_bf16 v[228:231], v[192:195], v[208:211], v[228:231]\n"
        "ds_read_b128 v[204:207], v247 offset:14336\n"
        "v_mfma_f32_16x16x32_bf16 v[232:235], v[196:199], v[208:211], v[232:235]\n"
        "s_waitcnt lgkmcnt(5)\n"
        "v_mfma_f32_16x16x32_bf16 v[212:215], v[176:179], v[56:59], v[212:215]\n"
        "ds_read_b64_tr_b16 v[192:193], v236 offset:61440\n"
        "v_mfma_f32_16x16x32_bf16 v[216:219], v[180:183], v[56:59], v[216:219]\n"
        "ds_read_b64_tr_b16 v[194:195], v236 offset:61952\n"
        "v_mfma_f32_16x16x32_bf16 v[220:223], v[176:179], v[128:131], v[220:223]\n"
        "ds_read_b64_tr_b16 v[196:197], v237 offset:61440\n"
        "v_mfma_f32_16x16x32_bf16 v[224:227], v[180:183], v[128:131], v[224:227]\n"
        "ds_read_b64_tr_b16 v[198:199], v237 offset:61952\n"
        "v_mfma_f32_16x16x32_bf16 v[228:231], v[176:179], v[200:203], v[228:231]\n"
        "ds_read_b128 v[208:211], v246 offset:16384\n"
        "v_mfma_f32_16x16x32_bf16 v[232:235], v[180:183], v[200:203], v[232:235]\n"
        "s_waitcnt lgkmcnt(5)\n"
        "v_mfma_f32_16x16x32_bf16 v[212:215], v[184:187], v[60:63], v[212:215]\n"
        "ds_read_b64_tr_b16 v[176:177], v236 offset:57344\n"
        "v_mfma_f32_16x16x32_bf16 v[216:219], v[188:191], v[60:63], v[216:219]\n"
        "ds_read_b64_tr_b16 v[178:179], v236 offset:57856\n"
        "v_mfma_f32_16x16x32_bf16 v[220:223], v[184:187], v[132:135], v[220:223]\n"
        "ds_read_b64_tr_b16 v[180:181], v237 offset:57344\n"
        "v_mfma_f32_16x16x32_bf16 v[224:227], v[188:191], v[132:135], v[224:227]\n"
        "ds_read_b64_tr_b16 v[182:183], v237 offset:57856\n"
        "v_mfma_f32_16x16x32_bf16 v[228:231], v[184:187], v[204:207], v[228:231]\n"
        "ds_read_b128 v[200:203], v247 offset:18432\n"
        "v_mfma_f32_16x16x32_bf16 v[232:235], v[188:191], v[204:207], v[232:235]\n"
        "s_waitcnt lgkmcnt(5)\n"
        "v_mfma_f32_16x16x32_bf16 v[212:215], v[192:195], v[64:67], v[212:215]\n"
        "ds_read_b64_tr_b16 v[184:185], v236 offset:36864\n"
        "v_mfma_f32_16x16x32_bf16 v[216:219], v[196:199], v[64:67], v[216:219]\n"
        "ds_read_b64_tr_b16 v[186:187], v236 offset:37376\n"
        "v_mfma_f32_16x16x32_bf16 v[220:223], v[192:195], v[136:139], v[220:223]\n"
        "ds_read_b64_tr_b16 v[188:189], v237 offset:36864\n"
        "v_mfma_f32_16x16x32_bf16 v[224:227], v[196:199], v[136:139], v[224:227]\n"
        "ds_read_b64_tr_b16 v[190:191], v237 offset:37376\n"
        "v_mfma_f32_16x16x32_bf16 v[228:231], v[192:195], v[208:211], v[228:231]\n"
        "ds_read_b128 v[204:207], v246 offset:20480\n"
        "v_mfma_f32_16x16x32_bf16 v[232:235], v[196:199], v[208:211], v[232:235]\n"
        "s_waitcnt lgkmcnt(5)\n"
        "v_mfma_f32_16x16x32_bf16 v[212:215], v[176:179], v[68:71], v[212:215]\n"
        "ds_read_b64_tr_b16 v[192:193], v236 offset:16384\n"
        "v_mfma_f32_16x16x32_bf16 v[216:219], v[180:183], v[68:71], v[216:219]\n"
        "ds_read_b64_tr_b16 v[194:195], v236 offset:16896\n"
        "v_mfma_f32_16x16x32_bf16 v[220:223], v[176:179], v[140:143], v[220:223]\n"
        "ds_read_b64_tr_b16 v[196:197], v237 offset:16384\n"
        "v_mfma_f32_16x16x32_bf16 v[224:227], v[180:183], v[140:143], v[224:227]\n"
        "ds_read_b64_tr_b16 v[198:199], v237 offset:16896\n"
        "v_mfma_f32_16x16x32_bf16 v[228:231], v[176:179], v[200:203], v[228:231]\n"
        "ds_read_b128 v[208:211], v247 offset:22528\n"
        "v_mfma_f32_16x16x32_bf16 v[232:235], v[180:183], v[200:203], v[232:235]\n"
        "s_waitcnt lgkmcnt(5)\n"
        "v_mfma_f32_16x16x32_bf16 v[212:215], v[184:187], v[72:75], v[212:215]\n"
        "ds_read_b64_tr_b16 v[176:177], v240 offset:20480\n"
        "v_mfma_f32_16x16x32_bf16 v[216:219], v[188:191], v[72:75], v[216:219]\n"
        "ds_read_b64_tr_b16 v[178:179], v240 offset:20992\n"
        "v_mfma_f32_16x16x32_bf16 v[220:223], v[184:187], v[144:147], v[220:223]\n"
        "ds_read_b64_tr_b16 v[180:181], v241 offset:20480\n"
        "v_mfma_f32_16x16x32_bf16 v[224:227], v[188:191], v[144:147], v[224:227]\n"
        "ds_read_b64_tr_b16 v[182:183], v241 offset:20992\n"
        "v_mfma_f32_16x16x32_bf16 v[228:231], v[184:187], v[204:207], v[228:231]\n"
        "ds_read_b128 v[200:203], v246 offset:24576\n"
        "v_mfma_f32_16x16x32_bf16 v[232:235], v[188:191], v[204:207], v[232:235]\n"
        "s_waitcnt lgkmcnt(5)\n"
        "v_mfma_f32_16x16x32_bf16 v[212:215], v[192:195], v[76:79], v[212:215]\n"
        "ds_read_b64_tr_b16 v[184:185], v240 offset:16384\n"
        "v_mfma_f32_16x16x32_bf16 v[216:219], v[196:199], v[76:79], v[216:219]\n"
        "ds_read_b64_tr_b16 v[186:187], v240 offset:16896\n"
        "v_mfma_f32_16x16x32_bf16 v[220:223], v[192:195], v[148:151], v[220:223]\n"
        "ds_read_b64_tr_b16 v[188:189], v241 offset:16384\n"
        "v_mfma_f32_16x16x32_bf16 v[224:227], v[196:199], v[148:151], v[224:227]\n"
        "ds_read_b64_tr_b16 v[190:191], v241 offset:16896\n"
        "v_mfma_f32_16x16x32_bf16 v[228:231], v[192:195], v[208:211], v[228:231]\n"
        "ds_read_b128 v[204:207], v247 offset:26624\n"
        "v_mfma_f32_16x16x32_bf16 v[232:235], v[196:199], v[208:211], v[232:235]\n"
        "s_waitcnt lgkmcnt(5)\n"
        "v_mfma_f32_16x16x32_bf16 v[212:215], v[176:179], v[80:83], v[212:215]\n"
        "ds_read_b64_tr_b16 v[192:193], v240 offset:12288\n"
        "v_mfma_f32_16x16x32_bf16 v[216:219], v[180:183], v[80:83], v[216:219]\n"
        "ds_read_b64_tr_b16 v[194:195], v240 offset:12800\n"
        "v_mfma_f32_16x16x32_bf16 v[220:223], v[176:179], v[152:155], v[220:223]\n"
        "ds_read_b64_tr_b16 v[196:197], v241 offset:12288\n"
        "v_mfma_f32_16x16x32_bf16 v[224:227], v[180:183], v[152:155], v[224:227]\n"
        "ds_read_b64_tr_b16 v[198:199], v241 offset:12800\n"
        "v_mfma_f32_16x16x32_bf16 v[228:231], v[176:179], v[200:203], v[228:231]\n"
        "ds_read_b128 v[208:211], v246 offset:28672\n"
        "v_mfma_f32_16x16x32_bf16 v[232:235], v[180:183], v[200:203], v[232:235]\n"
        "s_waitcnt lgkmcnt(5)\n"
        "v_mfma_f32_16x16x32_bf16 v[212:215], v[184:187], v[84:87], v[212:215]\n"
        "ds_read_b64_tr_b16 v[176:177], v240 offset:8192\n"
        "v_mfma_f32_16x16x32_bf16 v[216:219], v[188:191], v[84:87], v[216:219]\n"
        "ds_read_b64_tr_b16 v[178:179], v240 offset:8704\n"
        "v_mfma_f32_16x16x32_bf16 v[220:223], v[184:187], v[156:159], v[220:223]\n"
        "ds_read_b64_tr_b16 v[180:181], v241 offset:8192\n"
        "v_mfma_f32_16x16x32_bf16 v[224:227], v[188:191], v[156:159], v[224:227]\n"
        "ds_read_b64_tr_b16 v[182:183], v241 offset:8704\n"
        "v_mfma_f32_16x16x32_bf16 v[228:231], v[184:187], v[204:207], v[228:231]\n"
        "ds_read_b128 v[200:203], v247 offset:30720\n"
        "v_mfma_f32_16x16x32_bf16 v[232:235], v[188:191], v[204:207], v[232:235]\n"
        "s_waitcnt lgkmcnt(5)\n"
        "v_mfma_f32_16x16x32_bf16 v[212:215], v[192:195], v[88:91], v[212:215]\n"
        "ds_read_b64_tr_b16 v[184:185], v236 offset:12288\n"
        "v_mfma_f32_16x16x32_bf16 v[216:219], v[196:199], v[88:91], v[216:219]\n"
        "ds_read_b64_tr_b16 v[186:187], v236 offset:12800\n"
        "v_mfma_f32_16x16x32_bf16 v[220:223], v[192:195], v[160:163], v[220:223]\n"
        "ds_read_b64_tr_b16 v[188:189], v237 offset:12288\n"
        "v_mfma_f32_16x16x32_bf16 v[224:227], v[196:199], v[160:163], v[224:227]\n"
        "ds_read_b64_tr_b16 v[190:191], v237 offset:12800\n"
        "v_mfma_f32_16x16x32_bf16 v[228:231], v[192:195], v[208:211], v[228:231]\n"
        "ds_read_b128 v[204:207], v246 offset:32768\n"
        "v_mfma_f32_16x16x32_bf16 v[232:235], v[196:199], v[208:211], v[232:235]\n"
        "s_waitcnt lgkmcnt(5)\n"
        "v_mfma_f32_16x16x32_bf16 v[212:215], v[176:179], v[92:95], v[212:215]\n"
        "ds_read_b64_tr_b16 v[192:193], v236 offset:8192\n"
        "v_mfma_f32_16x16x32_bf16 v[216:219], v[180:183], v[92:95], v[216:219]\n"
        "ds_read_b64_tr_b16 v[194:195], v236 offset:8704\n"
        "v_mfma_f32_16x16x32_bf16 v[220:223], v[176:179], v[164:167], v[220:223]\n"
        "ds_read_b64_tr_b16 v[196:197], v237 offset:8192\n"
        "v_mfma_f32_16x16x32_bf16 v[224:227], v[180:183], v[164:167], v[224:227]\n"
        "ds_read_b64_tr_b16 v[198:199], v237 offset:8704\n"
        "v_mfma_f32_16x16x32_bf16 v[228:231], v[176:179], v[200:203], v[228:231]\n"
        "ds_read_b128 v[208:211], v247 offset:34816\n"
        "v_mfma_f32_16x16x32_bf16 v[232:235], v[180:183], v[200:203], v[232:235]\n"
        "s_waitcnt lgkmcnt(5)\n"
        "v_mfma_f32_16x16x32_bf16 v[212:215], v[184:187], v[96:99], v[212:215]\n"
        "v_mfma_f32_16x16x32_bf16 v[216:219], v[188:191], v[96:99], v[216:219]\n"
        "v_mfma_f32_16x16x32_bf16 v[220:223], v[184:187], v[168:171], v[220:223]\n"
        "v_mfma_f32_16x16x32_bf16 v[224:227], v[188:191], v[168:171], v[224:227]\n"
        "v_mfma_f32_16x16x32_bf16 v[228:231], v[184:187], v[204:207], v[228:231]\n"
        "v_mfma_f32_16x16x32_bf16 v[232:235], v[188:191], v[204:207], v[232:235]\n"
        "s_waitcnt lgkmcnt(0)\n"
        "v_mfma_f32_16x16x32_bf16 v[212:215], v[192:195], v[100:103], v[212:215]\n"
        "v_mfma_f32_16x16x32_bf16 v[216:219], v[196:199], v[100:103], v[216:219]\n"
        "v_mfma_f32_16x16x32_bf16 v[220:223], v[192:195], v[172:175], v[220:223]\n"
        "v_mfma_f32_16x16x32_bf16 v[224:227], v[196:199], v[172:175], v[224:227]\n"
        "v_mfma_f32_16x16x32_bf16 v[228:231], v[192:195], v[208:211], v[228:231]\n"
        "v_mfma_f32_16x16x32_bf16 v[232:235], v[196:199], v[208:211], v[232:235]\n"
        "s_nop 2\n"
        "v_cvt_pk_bf16_f32 v6, v212, v213\n"
        "v_cvt_pk_bf16_f32 v7, v214, v215\n"
        "ds_write_b64 v30, v[6:7] offset:0\n"
        "v_cvt_pk_bf16_f32 v8, v216, v217\n"
        "v_cvt_pk_bf16_f32 v9, v218, v219\n"
        "ds_write_b64 v31, v[8:9] offset:0\n"
        "v_cvt_pk_bf16_f32 v10, v220, v221\n"
        "v_cvt_pk_bf16_f32 v11, v222, v223\n"
        "ds_write_b64 v30, v[10:11] offset:4096\n"
        "v_cvt_pk_bf16_f32 v12, v224, v225\n"
        "v_cvt_pk_bf16_f32 v13, v226, v227\n"
        "ds_write_b64 v31, v[12:13] offset:4096\n"
        "v_cvt_pk_bf16_f32 v14, v228, v229\n"
        "v_cvt_pk_bf16_f32 v15, v230, v231\n"
        "ds_write_b64 v30, v[14:15] offset:20480\n"
        "v_cvt_pk_bf16_f32 v16, v232, v233\n"
        "v_cvt_pk_bf16_f32 v17, v234, v235\n"
        "ds_write_b64 v31, v[16:17] offset:20480\n"
        "s_waitcnt lgkmcnt(0)\n"
        "s_barrier\n"
        "s_branch L%=_end\n"
        "L%=_ahelper:\n"
        "s_waitcnt vmcnt(3) lgkmcnt(0)\n"
        "s_barrier\n"
        "s_add_u32 m0, s46, 159744\n"
        "s_add_u32 s51, s36, 368640\n"
        "buffer_load_dwordx4 v14, s[20:23], s51 offen lds\n"
        "s_add_u32 m0, s46, 114688\n"
        "s_add_u32 s51, s36, 405504\n"
        "buffer_load_dwordx4 v14, s[20:23], s51 offen lds\n"
        "s_add_u32 m0, s46, 110592\n"
        "s_add_u32 s51, s36, 442368\n"
        "buffer_load_dwordx4 v14, s[20:23], s51 offen lds\n"
        "v_bfe_u32 v3, v0, 3, 5\n"
        "v_and_b32 v53, 7, v0\n"
        "v_mul_u32_u24 v250, 1152, v3\n"
        "v_lshl_add_u32 v250, v53, 4, v250\n"
        "v_and_b32 v4, 7, v3\n"
        "v_xor_b32 v4, v4, v53\n"
        "v_lshlrev_b32 v4, 4, v4\n"
        "v_lshl_add_u32 v251, v3, 7, v4\n"
        "s_mov_b32 s26, 9437184\n"
        "s_mov_b32 s27, 0x20000\n"
        "s_waitcnt vmcnt(3)\n"
        "s_barrier\n"
        "s_add_u32 m0, s46, 131072\n"
        "s_add_u32 s51, s36, 479232\n"
        "buffer_load_dwordx4 v14, s[20:23], s51 offen lds\n"
        "s_add_u32 m0, s46, 126976\n"
        "s_add_u32 s51, s36, 516096\n"
        "buffer_load_dwordx4 v14, s[20:23], s51 offen lds\n"
        "s_add_u32 m0, s46, 122880\n"
        "s_add_u32 s51, s36, 552960\n"
        "buffer_load_dwordx4 v14, s[20:23], s51 offen lds\n"
        "s_add_u32 m0, s46, 118784\n"
        "s_add_u32 s51, s36, 589824\n"
        "buffer_load_dwordx4 v14, s[20:23], s51 offen lds\n"
        "s_add_u32 m0, s46, 12288\n"
        "s_add_u32 s51, s36, 626688\n"
        "buffer_load_dwordx4 v14, s[20:23], s51 offen lds\n"
        "s_add_u32 m0, s46, 8192\n"
        "s_add_u32 s51, s37, 0\n"
        "buffer_load_dwordx4 v14, s[20:23], s51 offen lds\n"
        "s_add_u32 m0, s46, 4096\n"
        "s_add_u32 s51, s37, 36864\n"
        "buffer_load_dwordx4 v14, s[20:23], s51 offen lds\n"
        "s_add_u32 m0, s46, 0\n"
        "s_add_u32 s51, s37, 73728\n"
        "buffer_load_dwordx4 v14, s[20:23], s51 offen lds\n"
        "s_waitcnt vmcnt(6)\n"
        "s_barrier\n"
        "s_add_u32 m0, s46, 151552\n"
        "s_add_u32 s51, s37, 110592\n"
        "buffer_load_dwordx4 v14, s[20:23], s51 offen lds\n"
        "s_add_u32 m0, s46, 147456\n"
        "s_add_u32 s51, s37, 147456\n"
        "buffer_load_dwordx4 v14, s[20:23], s51 offen lds\n"
        "s_add_u32 m0, s46, 143360\n"
        "s_add_u32 s51, s37, 184320\n"
        "buffer_load_dwordx4 v14, s[20:23], s51 offen lds\n"
        "s_add_u32 m0, s46, 139264\n"
        "s_add_u32 s51, s37, 221184\n"
        "buffer_load_dwordx4 v14, s[20:23], s51 offen lds\n"
        "s_add_u32 m0, s46, 135168\n"
        "s_add_u32 s51, s37, 258048\n"
        "buffer_load_dwordx4 v14, s[20:23], s51 offen lds\n"
        "s_add_u32 m0, s46, 32768\n"
        "s_add_u32 s51, s37, 294912\n"
        "buffer_load_dwordx4 v14, s[20:23], s51 offen lds\n"
        "s_add_u32 m0, s46, 28672\n"
        "s_add_u32 s51, s37, 331776\n"
        "buffer_load_dwordx4 v14, s[20:23], s51 offen lds\n"
        "s_add_u32 m0, s46, 24576\n"
        "s_add_u32 s51, s37, 368640\n"
        "buffer_load_dwordx4 v14, s[20:23], s51 offen lds\n"
        "s_add_u32 m0, s46, 20480\n"
        "s_add_u32 s51, s37, 405504\n"
        "buffer_load_dwordx4 v14, s[20:23], s51 offen lds\n"
        "s_waitcnt vmcnt(10)\n"
        "s_barrier\n"
        "s_add_u32 m0, s46, 159744\n"
        "s_add_u32 s51, s37, 442368\n"
        "buffer_load_dwordx4 v14, s[20:23], s51 offen lds\n"
        "s_add_u32 m0, s46, 155648\n"
        "s_add_u32 s51, s37, 479232\n"
        "buffer_load_dwordx4 v14, s[20:23], s51 offen lds\n"
        "s_add_u32 m0, s46, 131072\n"
        "s_add_u32 s51, s37, 516096\n"
        "buffer_load_dwordx4 v14, s[20:23], s51 offen lds\n"
        "s_add_u32 m0, s46, 114688\n"
        "s_add_u32 s51, s37, 552960\n"
        "buffer_load_dwordx4 v14, s[20:23], s51 offen lds\n"
        "s_add_u32 m0, s46, 110592\n"
        "s_add_u32 s51, s37, 589824\n"
        "buffer_load_dwordx4 v14, s[20:23], s51 offen lds\n"
        "s_add_u32 m0, s46, 53248\n"
        "s_add_u32 s51, s37, 626688\n"
        "buffer_load_dwordx4 v14, s[20:23], s51 offen lds\n"
        "s_add_u32 m0, s46, 49152\n"
        "s_add_u32 s51, s38, 0\n"
        "buffer_load_dwordx4 v14, s[20:23], s51 offen lds\n"
        "s_add_u32 m0, s46, 45056\n"
        "s_add_u32 s51, s38, 36864\n"
        "buffer_load_dwordx4 v14, s[20:23], s51 offen lds\n"
        "s_add_u32 m0, s46, 40960\n"
        "s_add_u32 s51, s38, 73728\n"
        "buffer_load_dwordx4 v14, s[20:23], s51 offen lds\n"
        "s_waitcnt vmcnt(13)\n"
        "s_barrier\n"
        "s_add_u32 m0, s46, 126976\n"
        "s_add_u32 s51, s38, 110592\n"
        "buffer_load_dwordx4 v14, s[20:23], s51 offen lds\n"
        "s_add_u32 m0, s46, 122880\n"
        "s_add_u32 s51, s38, 147456\n"
        "buffer_load_dwordx4 v14, s[20:23], s51 offen lds\n"
        "s_add_u32 m0, s46, 118784\n"
        "s_add_u32 s51, s38, 184320\n"
        "buffer_load_dwordx4 v14, s[20:23], s51 offen lds\n"
        "s_add_u32 m0, s46, 69632\n"
        "s_add_u32 s51, s38, 221184\n"
        "buffer_load_dwordx4 v14, s[20:23], s51 offen lds\n"
        "s_add_u32 m0, s46, 65536\n"
        "s_add_u32 s51, s38, 258048\n"
        "buffer_load_dwordx4 v14, s[20:23], s51 offen lds\n"
        "s_add_u32 m0, s46, 61440\n"
        "s_add_u32 s51, s38, 294912\n"
        "buffer_load_dwordx4 v14, s[20:23], s51 offen lds\n"
        "s_add_u32 m0, s46, 57344\n"
        "s_add_u32 s51, s38, 331776\n"
        "buffer_load_dwordx4 v14, s[20:23], s51 offen lds\n"
        "s_add_u32 m0, s46, 36864\n"
        "s_add_u32 s51, s38, 368640\n"
        "buffer_load_dwordx4 v14, s[20:23], s51 offen lds\n"
        "s_add_u32 m0, s46, 16384\n"
        "s_add_u32 s51, s38, 405504\n"
        "buffer_load_dwordx4 v14, s[20:23], s51 offen lds\n"
        "s_waitcnt vmcnt(16)\n"
        "s_barrier\n"
        "s_add_u32 m0, s46, 151552\n"
        "s_add_u32 s51, s38, 442368\n"
        "buffer_load_dwordx4 v14, s[20:23], s51 offen lds\n"
        "s_add_u32 m0, s46, 147456\n"
        "s_add_u32 s51, s38, 479232\n"
        "buffer_load_dwordx4 v14, s[20:23], s51 offen lds\n"
        "s_add_u32 m0, s46, 143360\n"
        "s_add_u32 s51, s38, 516096\n"
        "buffer_load_dwordx4 v14, s[20:23], s51 offen lds\n"
        "s_add_u32 m0, s46, 139264\n"
        "s_add_u32 s51, s38, 552960\n"
        "buffer_load_dwordx4 v14, s[20:23], s51 offen lds\n"
        "s_add_u32 m0, s46, 12288\n"
        "s_add_u32 s51, s38, 589824\n"
        "buffer_load_dwordx4 v14, s[20:23], s51 offen lds\n"
        "s_add_u32 m0, s46, 8192\n"
        "s_add_u32 s51, s38, 626688\n"
        "buffer_load_dwordx4 v14, s[20:23], s51 offen lds\n"
        "s_waitcnt vmcnt(16)\n"
        "s_barrier\n"
        "s_waitcnt vmcnt(12)\n"
        "s_barrier\n"
        "ds_read_b128 v[40:43], v251 offset:0\n"
        "ds_read_b128 v[44:47], v251 offset:4096\n"
        "ds_read_b128 v[48:51], v251 offset:20480\n"
        "s_add_u32 s51, s33, s36\n"
        "s_waitcnt lgkmcnt(2)\n"
        "buffer_store_dwordx4 v[40:43], v250, s[24:27], s51 offen sc1 nt\n"
        "s_add_u32 s51, s34, s36\n"
        "s_waitcnt lgkmcnt(1)\n"
        "buffer_store_dwordx4 v[44:47], v250, s[24:27], s51 offen sc1 nt\n"
        "s_add_u32 s51, s35, s36\n"
        "s_waitcnt lgkmcnt(0)\n"
        "buffer_store_dwordx4 v[48:51], v250, s[24:27], s51 offen sc1 nt\n"
        "s_waitcnt vmcnt(3)\n"
        "s_barrier\n"
        "ds_read_b128 v[56:59], v251 offset:24576\n"
        "ds_read_b128 v[60:63], v251 offset:28672\n"
        "ds_read_b128 v[64:67], v251 offset:32768\n"
        "s_add_u32 s51, s33, s37\n"
        "s_waitcnt lgkmcnt(2)\n"
        "buffer_store_dwordx4 v[56:59], v250, s[24:27], s51 offen sc1 nt\n"
        "s_add_u32 s51, s34, s37\n"
        "s_waitcnt lgkmcnt(1)\n"
        "buffer_store_dwordx4 v[60:63], v250, s[24:27], s51 offen sc1 nt\n"
        "s_add_u32 s51, s35, s37\n"
        "s_waitcnt lgkmcnt(0)\n"
        "buffer_store_dwordx4 v[64:67], v250, s[24:27], s51 offen sc1 nt\n"
        "s_barrier\n"
        "ds_read_b128 v[72:75], v251 offset:0\n"
        "ds_read_b128 v[76:79], v251 offset:4096\n"
        "ds_read_b128 v[80:83], v251 offset:20480\n"
        "s_add_u32 s51, s33, s38\n"
        "s_waitcnt lgkmcnt(2)\n"
        "buffer_store_dwordx4 v[72:75], v250, s[24:27], s51 offen sc1 nt\n"
        "s_add_u32 s51, s34, s38\n"
        "s_waitcnt lgkmcnt(1)\n"
        "buffer_store_dwordx4 v[76:79], v250, s[24:27], s51 offen sc1 nt\n"
        "s_add_u32 s51, s35, s38\n"
        "s_waitcnt lgkmcnt(0)\n"
        "buffer_store_dwordx4 v[80:83], v250, s[24:27], s51 offen sc1 nt\n"
        "s_branch L%=_end\n"
        "L%=_pathb:\n"
        "s_and_b32 s49, s29, 1\n"
        "s_bfe_u32 s30, s29, 0x10001\n"
        "s_lshl_b32 s50, s30, 1\n"
        "s_mul_i32 s30, s30, 73728\n"
        "s_add_u32 s48, s30, 9289728\n"
        "s_xor_b32 s29, s49, 0\n"
        "s_mul_i32 s29, s29, 36864\n"
        "s_add_u32 s33, s29, s48\n"
        "s_xor_b32 s29, s49, 1\n"
        "s_mul_i32 s29, s29, 36864\n"
        "s_add_u32 s34, s29, s48\n"
        "s_cmp_ge_u32 s28, 2\n"
        "s_waitcnt lgkmcnt(0)\n"
        "s_cselect_b64 s[12:13], s[16:17], s[12:13]\n"
        "s_cmp_lg_u32 s47, 0\n"
        "s_cbranch_scc1 L%=_bnogb\n"
        "buffer_load_dwordx4 v[26:29], v12, s[12:15], 0 offen\n"
        "L%=_bnogb:\n"
        "buffer_load_dwordx4 v[32:35], v5, s[8:11], s33 offen offset:0 nt\n"
        "buffer_load_dwordx4 v[36:39], v5, s[8:11], s33 offen offset:256 nt\n"
        "buffer_load_dwordx4 v[40:43], v5, s[8:11], s33 offen offset:512 nt\n"
        "buffer_load_dwordx4 v[44:47], v5, s[8:11], s33 offen offset:768 nt\n"
        "buffer_load_dwordx2 v[48:49], v6, s[8:11], s33 offen offset:1024 nt\n"
        "v_bfe_u32 v3, v0, 7, 1\n"
        "v_lshl_add_u32 v7, v3, 12, v12\n"
        "v_lshlrev_b32 v8, 4, v2\n"
        "v_and_b32 v3, 1, v2\n"
        "v_lshlrev_b32 v3, 11, v3\n"
        "v_lshrrev_b32 v4, 1, v2\n"
        "v_lshl_add_u32 v9, v4, 4, v3\n"
        "v_and_b32 v1, 15, v1\n"
        "v_and_b32 v3, 7, v2\n"
        "v_xor_b32 v3, v3, v1\n"
        "v_lshlrev_b32 v3, 4, v3\n"
        "v_and_b32 v16, 3, v2\n"
        "v_lshl_add_u32 v3, v16, 8, v3\n"
        "v_lshrrev_b32 v16, 2, v2\n"
        "s_lshl_b32 s29, s47, 11\n"
        "s_lshl_b32 s30, s47, 10\n"
        "s_add_u32 s30, s30, 73728\n"
        "v_lshl_add_u32 v10, v16, 12, v3\n"
        "v_add_u32 v10, s29, v10\n"
        "v_lshl_add_u32 v13, v16, 11, v3\n"
        "v_add_u32 v13, s30, v13\n"
        "v_xor_b32 v3, v4, v1\n"
        "v_lshlrev_b32 v3, 4, v3\n"
        "v_and_b32 v16, 3, v4\n"
        "v_lshl_add_u32 v3, v16, 8, v3\n"
        "v_and_b32 v16, 1, v2\n"
        "v_lshl_add_u32 v3, v16, 3, v3\n"
        "v_lshrrev_b32 v16, 3, v2\n"
        "s_add_u32 s29, s29, 65536\n"
        "s_add_u32 s30, s30, 32768\n"
        "v_lshl_add_u32 v11, v16, 12, v3\n"
        "v_add_u32 v11, s29, v11\n"
        "v_lshl_add_u32 v19, v16, 11, v3\n"
        "v_add_u32 v19, s30, v19\n"
        "v_bfe_u32 v3, v0, 3, 5\n"
        "v_and_b32 v4, 2, v3\n"
        "v_bfe_u32 v16, v3, 3, 1\n"
        "v_lshl_add_u32 v4, v16, 2, v4\n"
        "v_and_b32 v16, 7, v0\n"
        "v_xor_b32 v4, v4, v16\n"
        "v_mul_u32_u24 v3, 1152, v3\n"
        "v_lshl_add_u32 v14, v4, 4, v3\n"
        "v_mov_b32 v15, 0x3727c5ac\n"
        "s_add_u32 s29, s50, 0\n"
        "s_and_b32 s29, s29, 3\n"
        "s_mul_i32 s30, s49, 5\n"
        "s_add_u32 s29, s29, s30\n"
        "s_lshl_b32 s36, s29, 7\n"
        "s_add_u32 s29, s50, 1\n"
        "s_and_b32 s29, s29, 3\n"
        "s_mul_i32 s30, s49, 5\n"
        "s_add_u32 s29, s29, s30\n"
        "s_lshl_b32 s37, s29, 7\n"
        "s_add_u32 s29, s50, 2\n"
        "s_and_b32 s29, s29, 3\n"
        "s_mul_i32 s30, s49, 5\n"
        "s_add_u32 s29, s29, s30\n"
        "s_lshl_b32 s38, s29, 7\n"
        "s_add_u32 s29, s50, 3\n"
        "s_and_b32 s29, s29, 3\n"
        "s_mul_i32 s30, s49, 5\n"
        "s_add_u32 s29, s29, s30\n"
        "s_lshl_b32 s39, s29, 7\n"
        "s_movk_i32 s40, 512\n"
        "s_mov_b32 s22, 663552\n"
        "s_mov_b32 s23, 0x20000\n"
        "s_movk_i32 s41, 0x3f80\n"
        "s_mov_b32 s42, 1.0\n"
        "s_mov_b32 s43, 0x3f803f80\n"
        "s_mov_b32 s44, 0xbae38e39\n"
        "s_mov_b32 s45, 0x3ae38e39\n"
        "s_lshl_b32 s46, s46, 10\n"
        "s_lshl_b32 s29, s47, 12\n"
        "s_add_u32 s52, s29, s46\n"
        "s_mul_i32 s53, s47, 36864\n"
        "s_add_u32 s53, s53, s36\n"
        "v_add_u32 v7, 73728, v7\n"
        "v_add_u32 v8, 73728, v8\n"
        "v_add_u32 v9, 73728, v9\n"
        "s_cmp_lg_u32 s47, 0\n"
        "s_cbranch_scc1 L%=_blate\n"
        "v_bfe_u32 v1, v0, 4, 2\n"
        "v_bfe_u32 v3, v0, 2, 2\n"
        "v_and_b32 v4, 3, v0\n"
        "v_lshl_add_u32 v16, v1, 3, v3\n"
        "v_lshlrev_b32 v236, 7, v16\n"
        "v_and_b32 v16, 1, v4\n"
        "v_lshl_add_u32 v236, v16, 3, v236\n"
        "v_and_b32 v3, 2, v3\n"
        "v_and_b32 v17, 1, v1\n"
        "v_lshl_add_u32 v3, v17, 2, v3\n"
        "v_bfe_u32 v16, v0, 7, 1\n"
        "v_lshrrev_b32 v4, 1, v4\n"
        "v_lshl_add_u32 v4, v16, 2, v4\n"
        "v_xor_b32 v4, v4, v3\n"
        "v_lshl_add_u32 v236, v4, 4, v236\n"
        "v_xor_b32 v237, 32, v236\n"
        "v_add_u32 v238, 65536, v236\n"
        "v_add_u32 v239, 65536, v237\n"
        "v_add_u32 v240, 131072, v236\n"
        "v_add_u32 v241, 131072, v237\n"
        "v_bfe_u32 v3, v0, 6, 1\n"
        "v_xor_b32 v4, v2, v1\n"
        "v_lshlrev_b32 v4, 4, v4\n"
        "v_lshl_add_u32 v4, v1, 8, v4\n"
        "v_lshl_add_u32 v242, v3, 11, v4\n"
        "v_xor_b32 v243, 64, v242\n"
        "v_add_u32 v244, 0x10000, v242\n"
        "v_add_u32 v245, 0x10000, v243\n"
        "v_lshl_add_u32 v3, v3, 4, v2\n"
        "v_lshrrev_b32 v4, 1, v1\n"
        "v_lshl_add_u32 v4, v16, 2, v4\n"
        "v_and_b32 v16, 7, v2\n"
        "v_xor_b32 v4, v4, v16\n"
        "v_lshlrev_b32 v4, 4, v4\n"
        "v_lshl_add_u32 v4, v3, 7, v4\n"
        "v_lshl_add_u32 v30, v17, 3, v4\n"
        "v_xor_b32 v31, 32, v30\n"
        "s_waitcnt vmcnt(5)\n"
        "v_lshlrev_b32 v196, 16, v26\n"
        "v_and_b32 v197, 0xffff0000, v26\n"
        "v_lshlrev_b32 v198, 16, v27\n"
        "v_and_b32 v199, 0xffff0000, v27\n"
        "v_lshlrev_b32 v200, 16, v28\n"
        "v_and_b32 v201, 0xffff0000, v28\n"
        "v_lshlrev_b32 v202, 16, v29\n"
        "v_and_b32 v203, 0xffff0000, v29\n"
        "ds_write_b128 v7, v[196:199]\n"
        "ds_write_b128 v7, v[200:203] offset:2048\n"
        "s_waitcnt lgkmcnt(0)\n"
        "s_branch L%=_bln\n"
        "L%=_blate:\n"
        "L%=_bln:\n"
        "s_barrier\n"
        "buffer_load_dwordx4 v[50:53], v5, s[8:11], s34 offen offset:0 nt\n"
        "buffer_load_dwordx4 v[54:57], v5, s[8:11], s34 offen offset:256 nt\n"
        "buffer_load_dwordx4 v[58:61], v5, s[8:11], s34 offen offset:512 nt\n"
        "buffer_load_dwordx4 v[62:65], v5, s[8:11], s34 offen offset:768 nt\n"
        "buffer_load_dwordx2 v[66:67], v6, s[8:11], s34 offen offset:1024 nt\n"
        "s_waitcnt vmcnt(5)\n"
        "s_setprio 2\n"
        "v_mov_b32 v16, 0\n"
        "v_mov_b32 v17, 0\n"
        "v_dot2c_f32_bf16 v16, s43, v32\n"
        "v_dot2c_f32_bf16 v17, s43, v33\n"
        "v_dot2c_f32_bf16 v16, s43, v34\n"
        "v_dot2c_f32_bf16 v17, s43, v35\n"
        "v_dot2c_f32_bf16 v16, s43, v36\n"
        "v_dot2c_f32_bf16 v17, s43, v37\n"
        "v_dot2c_f32_bf16 v16, s43, v38\n"
        "v_dot2c_f32_bf16 v17, s43, v39\n"
        "v_dot2c_f32_bf16 v16, s43, v40\n"
        "v_dot2c_f32_bf16 v17, s43, v41\n"
        "v_dot2c_f32_bf16 v16, s43, v42\n"
        "v_dot2c_f32_bf16 v17, s43, v43\n"
        "v_dot2c_f32_bf16 v16, s43, v44\n"
        "v_dot2c_f32_bf16 v17, s43, v45\n"
        "v_dot2c_f32_bf16 v16, s43, v46\n"
        "v_dot2c_f32_bf16 v17, s43, v47\n"
        "v_dot2c_f32_bf16 v16, s43, v48\n"
        "v_dot2c_f32_bf16 v17, s43, v49\n"
        "s_nop 2\n"
        "v_add_f32 v18, v16, v17\n"
        "s_nop 1\n"
        "v_add_f32_dpp v18, v18, v18 row_ror:8 row_mask:0xf bank_mask:0xf\n"
        "s_nop 1\n"
        "v_add_f32_dpp v18, v18, v18 row_ror:4 row_mask:0xf bank_mask:0xf\n"
        "s_nop 1\n"
        "v_add_f32_dpp v18, v18, v18 row_ror:2 row_mask:0xf bank_mask:0xf\n"
        "s_nop 1\n"
        "v_add_f32_dpp v18, v18, v18 row_ror:1 row_mask:0xf bank_mask:0xf\n"
        "v_mul_f32 v18, s44, v18\n"
        "v_cvt_pk_bf16_f32 v205, 0, v18\n"
        "v_sub_f32 v204, v18, v205\n"
        "v_mov_b32 v18, v205\n"
        "v_mul_f32 v204, v204, v204\n"
        "v_dot2_f32_bf16 v158, v32, s41, v18\n"
        "v_dot2_f32_bf16 v159, v32, s42, v18\n"
        "ds_read_b128 v[86:89], v8 offset:0\n"
        "v_dot2_f32_bf16 v160, v33, s41, v18\n"
        "v_dot2_f32_bf16 v161, v33, s42, v18\n"
        "ds_read_b128 v[90:93], v8 offset:2048\n"
        "v_dot2_f32_bf16 v162, v34, s41, v18\n"
        "v_dot2_f32_bf16 v163, v34, s42, v18\n"
        "ds_read_b128 v[94:97], v8 offset:256\n"
        "v_dot2_f32_bf16 v164, v35, s41, v18\n"
        "v_dot2_f32_bf16 v165, v35, s42, v18\n"
        "ds_read_b128 v[98:101], v8 offset:2304\n"
        "v_dot2_f32_bf16 v166, v36, s41, v18\n"
        "v_dot2_f32_bf16 v167, v36, s42, v18\n"
        "ds_read_b128 v[102:105], v8 offset:512\n"
        "v_dot2_f32_bf16 v168, v37, s41, v18\n"
        "v_dot2_f32_bf16 v169, v37, s42, v18\n"
        "ds_read_b128 v[106:109], v8 offset:2560\n"
        "v_dot2_f32_bf16 v170, v38, s41, v18\n"
        "v_dot2_f32_bf16 v171, v38, s42, v18\n"
        "ds_read_b128 v[110:113], v8 offset:768\n"
        "v_dot2_f32_bf16 v172, v39, s41, v18\n"
        "v_dot2_f32_bf16 v173, v39, s42, v18\n"
        "ds_read_b128 v[114:117], v8 offset:2816\n"
        "v_dot2_f32_bf16 v174, v40, s41, v18\n"
        "v_dot2_f32_bf16 v175, v40, s42, v18\n"
        "ds_read_b128 v[118:121], v9 offset:1024\n"
        "v_dot2_f32_bf16 v176, v41, s41, v18\n"
        "v_dot2_f32_bf16 v177, v41, s42, v18\n"
        "ds_read_b128 v[122:125], v8 offset:4096\n"
        "v_dot2_f32_bf16 v178, v42, s41, v18\n"
        "v_dot2_f32_bf16 v179, v42, s42, v18\n"
        "ds_read_b128 v[126:129], v8 offset:6144\n"
        "v_dot2_f32_bf16 v180, v43, s41, v18\n"
        "v_dot2_f32_bf16 v181, v43, s42, v18\n"
        "ds_read_b128 v[130:133], v8 offset:4352\n"
        "v_dot2_f32_bf16 v182, v44, s41, v18\n"
        "v_dot2_f32_bf16 v183, v44, s42, v18\n"
        "ds_read_b128 v[134:137], v8 offset:6400\n"
        "v_dot2_f32_bf16 v184, v45, s41, v18\n"
        "v_dot2_f32_bf16 v185, v45, s42, v18\n"
        "ds_read_b128 v[138:141], v8 offset:4608\n"
        "v_dot2_f32_bf16 v186, v46, s41, v18\n"
        "v_dot2_f32_bf16 v187, v46, s42, v18\n"
        "ds_read_b128 v[142:145], v8 offset:6656\n"
        "v_dot2_f32_bf16 v188, v47, s41, v18\n"
        "v_dot2_f32_bf16 v189, v47, s42, v18\n"
        "ds_read_b128 v[146:149], v8 offset:4864\n"
        "v_dot2_f32_bf16 v190, v48, s41, v18\n"
        "v_dot2_f32_bf16 v191, v48, s42, v18\n"
        "ds_read_b128 v[150:153], v8 offset:6912\n"
        "v_dot2_f32_bf16 v192, v49, s41, v18\n"
        "v_dot2_f32_bf16 v193, v49, s42, v18\n"
        "ds_read_b128 v[154:157], v9 offset:5120\n"
        "v_pk_mul_f32 v[22:23], v[158:159], v[158:159]\n"
        "v_pk_mul_f32 v[24:25], v[160:161], v[160:161]\n"
        "v_pk_fma_f32 v[22:23], v[162:163], v[162:163], v[22:23]\n"
        "v_pk_fma_f32 v[24:25], v[164:165], v[164:165], v[24:25]\n"
        "v_pk_fma_f32 v[22:23], v[166:167], v[166:167], v[22:23]\n"
        "v_pk_fma_f32 v[24:25], v[168:169], v[168:169], v[24:25]\n"
        "v_pk_fma_f32 v[22:23], v[170:171], v[170:171], v[22:23]\n"
        "v_pk_fma_f32 v[24:25], v[172:173], v[172:173], v[24:25]\n"
        "v_pk_fma_f32 v[22:23], v[174:175], v[174:175], v[22:23]\n"
        "v_pk_fma_f32 v[24:25], v[176:177], v[176:177], v[24:25]\n"
        "v_pk_fma_f32 v[22:23], v[178:179], v[178:179], v[22:23]\n"
        "v_pk_fma_f32 v[24:25], v[180:181], v[180:181], v[24:25]\n"
        "v_pk_fma_f32 v[22:23], v[182:183], v[182:183], v[22:23]\n"
        "v_pk_fma_f32 v[24:25], v[184:185], v[184:185], v[24:25]\n"
        "v_pk_fma_f32 v[22:23], v[186:187], v[186:187], v[22:23]\n"
        "v_pk_fma_f32 v[24:25], v[188:189], v[188:189], v[24:25]\n"
        "v_pk_fma_f32 v[22:23], v[190:191], v[190:191], v[22:23]\n"
        "v_pk_fma_f32 v[24:25], v[192:193], v[192:193], v[24:25]\n"
        "s_waitcnt lgkmcnt(0)\n"
        "v_pk_mul_f32 v[158:159], v[158:159], v[86:87]\n"
        "v_pk_mul_f32 v[160:161], v[160:161], v[88:89]\n"
        "v_pk_add_f32 v[22:23], v[22:23], v[24:25]\n"
        "v_pk_mul_f32 v[162:163], v[162:163], v[90:91]\n"
        "v_pk_mul_f32 v[164:165], v[164:165], v[92:93]\n"
        "v_add_f32 v20, v22, v23\n"
        "v_pk_mul_f32 v[166:167], v[166:167], v[94:95]\n"
        "v_pk_mul_f32 v[168:169], v[168:169], v[96:97]\n"
        "v_add_f32_dpp v20, v20, v20 row_ror:8 row_mask:0xf bank_mask:0xf\n"
        "v_pk_mul_f32 v[170:171], v[170:171], v[98:99]\n"
        "v_pk_mul_f32 v[172:173], v[172:173], v[100:101]\n"
        "v_add_f32_dpp v20, v20, v20 row_ror:4 row_mask:0xf bank_mask:0xf\n"
        "v_pk_mul_f32 v[174:175], v[174:175], v[102:103]\n"
        "v_pk_mul_f32 v[176:177], v[176:177], v[104:105]\n"
        "v_add_f32_dpp v20, v20, v20 row_ror:2 row_mask:0xf bank_mask:0xf\n"
        "v_pk_mul_f32 v[178:179], v[178:179], v[106:107]\n"
        "v_pk_mul_f32 v[180:181], v[180:181], v[108:109]\n"
        "v_add_f32_dpp v20, v20, v20 row_ror:1 row_mask:0xf bank_mask:0xf\n"
        "v_pk_mul_f32 v[182:183], v[182:183], v[110:111]\n"
        "v_fma_f32 v20, v20, s45, -v204\n"
        "v_pk_mul_f32 v[184:185], v[184:185], v[112:113]\n"
        "v_max_f32 v20, 0, v20\n"
        "v_pk_mul_f32 v[186:187], v[186:187], v[114:115]\n"
        "v_cvt_pk_bf16_f32 v20, 0, v20\n"
        "v_pk_mul_f32 v[188:189], v[188:189], v[116:117]\n"
        "v_add_f32 v20, v20, v15\n"
        "v_pk_mul_f32 v[190:191], v[190:191], v[118:119]\n"
        "v_cvt_pk_bf16_f32 v20, 0, v20\n"
        "v_pk_mul_f32 v[192:193], v[192:193], v[120:121]\n"
        "v_sqrt_f32 v20, v20\n"
        "s_nop 0\n"
        "v_cvt_pk_bf16_f32 v20, 0, v20\n"
        "v_rcp_f32 v20, v20\n"
        "s_waitcnt vmcnt(0)\n"
        "v_mov_b32 v16, 0\n"
        "v_mov_b32 v17, 0\n"
        "v_dot2c_f32_bf16 v16, s43, v50\n"
        "v_dot2c_f32_bf16 v17, s43, v51\n"
        "v_dot2c_f32_bf16 v16, s43, v52\n"
        "v_dot2c_f32_bf16 v17, s43, v53\n"
        "v_dot2c_f32_bf16 v16, s43, v54\n"
        "v_dot2c_f32_bf16 v17, s43, v55\n"
        "v_dot2c_f32_bf16 v16, s43, v56\n"
        "v_dot2c_f32_bf16 v17, s43, v57\n"
        "v_dot2c_f32_bf16 v16, s43, v58\n"
        "v_dot2c_f32_bf16 v17, s43, v59\n"
        "v_dot2c_f32_bf16 v16, s43, v60\n"
        "v_dot2c_f32_bf16 v17, s43, v61\n"
        "v_dot2c_f32_bf16 v16, s43, v62\n"
        "v_dot2c_f32_bf16 v17, s43, v63\n"
        "v_dot2c_f32_bf16 v16, s43, v64\n"
        "v_dot2c_f32_bf16 v17, s43, v65\n"
        "v_dot2c_f32_bf16 v16, s43, v66\n"
        "v_dot2c_f32_bf16 v17, s43, v67\n"
        "v_pk_fma_f32 v[158:159], v[158:159], v[20:21], v[122:123] op_sel_hi:[1,0,1]\n"
        "v_pk_fma_f32 v[160:161], v[160:161], v[20:21], v[124:125] op_sel_hi:[1,0,1]\n"
        "v_pk_fma_f32 v[162:163], v[162:163], v[20:21], v[126:127] op_sel_hi:[1,0,1]\n"
        "v_cvt_pk_bf16_f32 v32, v158, v159\n"
        "v_pk_fma_f32 v[164:165], v[164:165], v[20:21], v[128:129] op_sel_hi:[1,0,1]\n"
        "v_cvt_pk_bf16_f32 v33, v160, v161\n"
        "v_add_f32 v18, v16, v17\n"
        "v_pk_fma_f32 v[166:167], v[166:167], v[20:21], v[130:131] op_sel_hi:[1,0,1]\n"
        "v_cvt_pk_bf16_f32 v34, v162, v163\n"
        "v_pk_fma_f32 v[168:169], v[168:169], v[20:21], v[132:133] op_sel_hi:[1,0,1]\n"
        "v_add_f32_dpp v18, v18, v18 row_ror:8 row_mask:0xf bank_mask:0xf\n"
        "v_cvt_pk_bf16_f32 v35, v164, v165\n"
        "ds_write_b128 v10, v[32:35] offset:0\n"
        "v_pk_fma_f32 v[170:171], v[170:171], v[20:21], v[134:135] op_sel_hi:[1,0,1]\n"
        "v_add_f32_dpp v18, v18, v18 row_ror:4 row_mask:0xf bank_mask:0xf\n"
        "v_cvt_pk_bf16_f32 v36, v166, v167\n"
        "v_pk_fma_f32 v[172:173], v[172:173], v[20:21], v[136:137] op_sel_hi:[1,0,1]\n"
        "s_add_u32 m0, s52, 81920\n"
        "s_add_u32 s51, s53, 0\n"
        "buffer_load_dwordx4 v14, s[20:23], s51 offen lds\n"
        "v_cvt_pk_bf16_f32 v37, v168, v169\n"
        "v_add_f32_dpp v18, v18, v18 row_ror:2 row_mask:0xf bank_mask:0xf\n"
        "v_pk_fma_f32 v[174:175], v[174:175], v[20:21], v[138:139] op_sel_hi:[1,0,1]\n"
        "v_cvt_pk_bf16_f32 v38, v170, v171\n"
        "v_pk_fma_f32 v[176:177], v[176:177], v[20:21], v[140:141] op_sel_hi:[1,0,1]\n"
        "v_add_f32_dpp v18, v18, v18 row_ror:1 row_mask:0xf bank_mask:0xf\n"
        "v_cvt_pk_bf16_f32 v39, v172, v173\n"
        "ds_write_b128 v10, v[36:39] offset:16384\n"
        "v_pk_fma_f32 v[178:179], v[178:179], v[20:21], v[142:143] op_sel_hi:[1,0,1]\n"
        "v_mul_f32 v18, s44, v18\n"
        "v_cvt_pk_bf16_f32 v40, v174, v175\n"
        "v_pk_fma_f32 v[180:181], v[180:181], v[20:21], v[144:145] op_sel_hi:[1,0,1]\n"
        "v_cvt_pk_bf16_f32 v41, v176, v177\n"
        "v_cvt_pk_bf16_f32 v205, 0, v18\n"
        "v_pk_fma_f32 v[182:183], v[182:183], v[20:21], v[146:147] op_sel_hi:[1,0,1]\n"
        "s_add_u32 m0, s52, 90112\n"
        "s_add_u32 s51, s53, 73728\n"
        "buffer_load_dwordx4 v14, s[20:23], s51 offen lds\n"
        "v_cvt_pk_bf16_f32 v42, v178, v179\n"
        "v_pk_fma_f32 v[184:185], v[184:185], v[20:21], v[148:149] op_sel_hi:[1,0,1]\n"
        "v_sub_f32 v204, v18, v205\n"
        "v_cvt_pk_bf16_f32 v43, v180, v181\n"
        "ds_write_b128 v10, v[40:43] offset:32768\n"
        "v_pk_fma_f32 v[186:187], v[186:187], v[20:21], v[150:151] op_sel_hi:[1,0,1]\n"
        "v_mov_b32 v18, v205\n"
        "v_cvt_pk_bf16_f32 v44, v182, v183\n"
        "v_pk_fma_f32 v[188:189], v[188:189], v[20:21], v[152:153] op_sel_hi:[1,0,1]\n"
        "v_cvt_pk_bf16_f32 v45, v184, v185\n"
        "v_mul_f32 v204, v204, v204\n"
        "v_pk_fma_f32 v[190:191], v[190:191], v[20:21], v[154:155] op_sel_hi:[1,0,1]\n"
        "v_cvt_pk_bf16_f32 v46, v186, v187\n"
        "v_pk_fma_f32 v[192:193], v[192:193], v[20:21], v[156:157] op_sel_hi:[1,0,1]\n"
        "v_cvt_pk_bf16_f32 v47, v188, v189\n"
        "s_add_u32 m0, s52, 98304\n"
        "s_add_u32 s51, s53, 147456\n"
        "buffer_load_dwordx4 v14, s[20:23], s51 offen lds\n"
        "ds_write_b128 v10, v[44:47] offset:49152\n"
        "v_cvt_pk_bf16_f32 v48, v190, v191\n"
        "v_cvt_pk_bf16_f32 v49, v192, v193\n"
        "ds_write_b64 v11, v[48:49] offset:0\n"
        "s_setprio 1\n"
        "v_dot2_f32_bf16 v158, v50, s41, v18\n"
        "v_dot2_f32_bf16 v159, v50, s42, v18\n"
        "v_dot2_f32_bf16 v160, v51, s41, v18\n"
        "v_dot2_f32_bf16 v161, v51, s42, v18\n"
        "v_dot2_f32_bf16 v162, v52, s41, v18\n"
        "v_dot2_f32_bf16 v163, v52, s42, v18\n"
        "v_dot2_f32_bf16 v164, v53, s41, v18\n"
        "v_dot2_f32_bf16 v165, v53, s42, v18\n"
        "v_dot2_f32_bf16 v166, v54, s41, v18\n"
        "v_dot2_f32_bf16 v167, v54, s42, v18\n"
        "v_dot2_f32_bf16 v168, v55, s41, v18\n"
        "v_dot2_f32_bf16 v169, v55, s42, v18\n"
        "v_dot2_f32_bf16 v170, v56, s41, v18\n"
        "v_dot2_f32_bf16 v171, v56, s42, v18\n"
        "v_dot2_f32_bf16 v172, v57, s41, v18\n"
        "v_dot2_f32_bf16 v173, v57, s42, v18\n"
        "v_dot2_f32_bf16 v174, v58, s41, v18\n"
        "v_dot2_f32_bf16 v175, v58, s42, v18\n"
        "s_add_u32 m0, s52, 106496\n"
        "s_add_u32 s51, s53, 221184\n"
        "buffer_load_dwordx4 v14, s[20:23], s51 offen lds\n"
        "v_dot2_f32_bf16 v176, v59, s41, v18\n"
        "v_dot2_f32_bf16 v177, v59, s42, v18\n"
        "v_dot2_f32_bf16 v178, v60, s41, v18\n"
        "v_dot2_f32_bf16 v179, v60, s42, v18\n"
        "v_dot2_f32_bf16 v180, v61, s41, v18\n"
        "v_dot2_f32_bf16 v181, v61, s42, v18\n"
        "v_dot2_f32_bf16 v182, v62, s41, v18\n"
        "v_dot2_f32_bf16 v183, v62, s42, v18\n"
        "v_dot2_f32_bf16 v184, v63, s41, v18\n"
        "v_dot2_f32_bf16 v185, v63, s42, v18\n"
        "v_dot2_f32_bf16 v186, v64, s41, v18\n"
        "v_dot2_f32_bf16 v187, v64, s42, v18\n"
        "v_dot2_f32_bf16 v188, v65, s41, v18\n"
        "v_dot2_f32_bf16 v189, v65, s42, v18\n"
        "v_dot2_f32_bf16 v190, v66, s41, v18\n"
        "v_dot2_f32_bf16 v191, v66, s42, v18\n"
        "s_add_u32 m0, s52, 114688\n"
        "s_add_u32 s51, s53, 294912\n"
        "buffer_load_dwordx4 v14, s[20:23], s51 offen lds\n"
        "v_dot2_f32_bf16 v192, v67, s41, v18\n"
        "v_dot2_f32_bf16 v193, v67, s42, v18\n"
        "v_pk_mul_f32 v[22:23], v[158:159], v[158:159]\n"
        "v_pk_mul_f32 v[24:25], v[160:161], v[160:161]\n"
        "v_pk_fma_f32 v[22:23], v[162:163], v[162:163], v[22:23]\n"
        "v_pk_fma_f32 v[24:25], v[164:165], v[164:165], v[24:25]\n"
        "v_pk_fma_f32 v[22:23], v[166:167], v[166:167], v[22:23]\n"
        "v_pk_fma_f32 v[24:25], v[168:169], v[168:169], v[24:25]\n"
        "v_pk_fma_f32 v[22:23], v[170:171], v[170:171], v[22:23]\n"
        "v_pk_fma_f32 v[24:25], v[172:173], v[172:173], v[24:25]\n"
        "v_pk_fma_f32 v[22:23], v[174:175], v[174:175], v[22:23]\n"
        "v_pk_fma_f32 v[24:25], v[176:177], v[176:177], v[24:25]\n"
        "v_pk_fma_f32 v[22:23], v[178:179], v[178:179], v[22:23]\n"
        "v_pk_fma_f32 v[24:25], v[180:181], v[180:181], v[24:25]\n"
        "v_pk_fma_f32 v[22:23], v[182:183], v[182:183], v[22:23]\n"
        "v_pk_fma_f32 v[24:25], v[184:185], v[184:185], v[24:25]\n"
        "s_add_u32 m0, s52, 122880\n"
        "s_add_u32 s51, s53, 368640\n"
        "buffer_load_dwordx4 v14, s[20:23], s51 offen lds\n"
        "v_pk_fma_f32 v[22:23], v[186:187], v[186:187], v[22:23]\n"
        "v_pk_fma_f32 v[24:25], v[188:189], v[188:189], v[24:25]\n"
        "v_pk_fma_f32 v[22:23], v[190:191], v[190:191], v[22:23]\n"
        "v_pk_fma_f32 v[24:25], v[192:193], v[192:193], v[24:25]\n"
        "v_pk_mul_f32 v[158:159], v[158:159], v[86:87]\n"
        "v_pk_mul_f32 v[160:161], v[160:161], v[88:89]\n"
        "v_pk_mul_f32 v[162:163], v[162:163], v[90:91]\n"
        "v_pk_add_f32 v[22:23], v[22:23], v[24:25]\n"
        "v_pk_mul_f32 v[164:165], v[164:165], v[92:93]\n"
        "v_pk_mul_f32 v[166:167], v[166:167], v[94:95]\n"
        "v_add_f32 v20, v22, v23\n"
        "v_pk_mul_f32 v[168:169], v[168:169], v[96:97]\n"
        "v_pk_mul_f32 v[170:171], v[170:171], v[98:99]\n"
        "v_add_f32_dpp v20, v20, v20 row_ror:8 row_mask:0xf bank_mask:0xf\n"
        "v_pk_mul_f32 v[172:173], v[172:173], v[100:101]\n"
        "v_pk_mul_f32 v[174:175], v[174:175], v[102:103]\n"
        "v_add_f32_dpp v20, v20, v20 row_ror:4 row_mask:0xf bank_mask:0xf\n"
        "v_pk_mul_f32 v[176:177], v[176:177], v[104:105]\n"
        "v_pk_mul_f32 v[178:179], v[178:179], v[106:107]\n"
        "v_add_f32_dpp v20, v20, v20 row_ror:2 row_mask:0xf bank_mask:0xf\n"
        "v_pk_mul_f32 v[180:181], v[180:181], v[108:109]\n"
        "v_pk_mul_f32 v[182:183], v[182:183], v[110:111]\n"
        "v_add_f32_dpp v20, v20, v20 row_ror:1 row_mask:0xf bank_mask:0xf\n"
        "v_pk_mul_f32 v[184:185], v[184:185], v[112:113]\n"
        "v_fma_f32 v20, v20, s45, -v204\n"
        "v_pk_mul_f32 v[186:187], v[186:187], v[114:115]\n"
        "v_max_f32 v20, 0, v20\n"
        "v_pk_mul_f32 v[188:189], v[188:189], v[116:117]\n"
        "v_cvt_pk_bf16_f32 v20, 0, v20\n"
        "v_pk_mul_f32 v[190:191], v[190:191], v[118:119]\n"
        "v_add_f32 v20, v20, v15\n"
        "v_pk_mul_f32 v[192:193], v[192:193], v[120:121]\n"
        "v_cvt_pk_bf16_f32 v20, 0, v20\n"
        "v_sqrt_f32 v20, v20\n"
        "s_nop 0\n"
        "v_cvt_pk_bf16_f32 v20, 0, v20\n"
        "v_rcp_f32 v20, v20\n"
        "s_nop 0\n"
        "v_pk_fma_f32 v[158:159], v[158:159], v[20:21], v[122:123] op_sel_hi:[1,0,1]\n"
        "v_pk_fma_f32 v[160:161], v[160:161], v[20:21], v[124:125] op_sel_hi:[1,0,1]\n"
        "v_pk_fma_f32 v[162:163], v[162:163], v[20:21], v[126:127] op_sel_hi:[1,0,1]\n"
        "v_cvt_pk_bf16_f32 v50, v158, v159\n"
        "v_pk_fma_f32 v[164:165], v[164:165], v[20:21], v[128:129] op_sel_hi:[1,0,1]\n"
        "v_cvt_pk_bf16_f32 v51, v160, v161\n"
        "v_pk_fma_f32 v[166:167], v[166:167], v[20:21], v[130:131] op_sel_hi:[1,0,1]\n"
        "v_cvt_pk_bf16_f32 v52, v162, v163\n"
        "v_pk_fma_f32 v[168:169], v[168:169], v[20:21], v[132:133] op_sel_hi:[1,0,1]\n"
        "v_cvt_pk_bf16_f32 v53, v164, v165\n"
        "ds_write_b128 v10, v[50:53] offset:1024\n"
        "v_pk_fma_f32 v[170:171], v[170:171], v[20:21], v[134:135] op_sel_hi:[1,0,1]\n"
        "v_cvt_pk_bf16_f32 v54, v166, v167\n"
        "v_pk_fma_f32 v[172:173], v[172:173], v[20:21], v[136:137] op_sel_hi:[1,0,1]\n"
        "s_add_u32 m0, s52, 131072\n"
        "s_add_u32 s51, s53, 442368\n"
        "buffer_load_dwordx4 v14, s[20:23], s51 offen lds\n"
        "v_cvt_pk_bf16_f32 v55, v168, v169\n"
        "v_pk_fma_f32 v[174:175], v[174:175], v[20:21], v[138:139] op_sel_hi:[1,0,1]\n"
        "v_cvt_pk_bf16_f32 v56, v170, v171\n"
        "v_pk_fma_f32 v[176:177], v[176:177], v[20:21], v[140:141] op_sel_hi:[1,0,1]\n"
        "v_cvt_pk_bf16_f32 v57, v172, v173\n"
        "ds_write_b128 v10, v[54:57] offset:17408\n"
        "v_pk_fma_f32 v[178:179], v[178:179], v[20:21], v[142:143] op_sel_hi:[1,0,1]\n"
        "v_cvt_pk_bf16_f32 v58, v174, v175\n"
        "v_pk_fma_f32 v[180:181], v[180:181], v[20:21], v[144:145] op_sel_hi:[1,0,1]\n"
        "v_cvt_pk_bf16_f32 v59, v176, v177\n"
        "v_pk_fma_f32 v[182:183], v[182:183], v[20:21], v[146:147] op_sel_hi:[1,0,1]\n"
        "v_cvt_pk_bf16_f32 v60, v178, v179\n"
        "s_add_u32 m0, s52, 139264\n"
        "s_add_u32 s51, s53, 516096\n"
        "buffer_load_dwordx4 v14, s[20:23], s51 offen lds\n"
        "v_pk_fma_f32 v[184:185], v[184:185], v[20:21], v[148:149] op_sel_hi:[1,0,1]\n"
        "v_cvt_pk_bf16_f32 v61, v180, v181\n"
        "ds_write_b128 v10, v[58:61] offset:33792\n"
        "v_pk_fma_f32 v[186:187], v[186:187], v[20:21], v[150:151] op_sel_hi:[1,0,1]\n"
        "v_cvt_pk_bf16_f32 v62, v182, v183\n"
        "v_pk_fma_f32 v[188:189], v[188:189], v[20:21], v[152:153] op_sel_hi:[1,0,1]\n"
        "v_cvt_pk_bf16_f32 v63, v184, v185\n"
        "v_pk_fma_f32 v[190:191], v[190:191], v[20:21], v[154:155] op_sel_hi:[1,0,1]\n"
        "v_cvt_pk_bf16_f32 v64, v186, v187\n"
        "v_pk_fma_f32 v[192:193], v[192:193], v[20:21], v[156:157] op_sel_hi:[1,0,1]\n"
        "v_cvt_pk_bf16_f32 v65, v188, v189\n"
        "ds_write_b128 v10, v[62:65] offset:50176\n"
        "s_add_u32 m0, s52, 147456\n"
        "s_add_u32 s51, s53, 589824\n"
        "buffer_load_dwordx4 v14, s[20:23], s51 offen lds\n"
        "v_cvt_pk_bf16_f32 v66, v190, v191\n"
        "v_cvt_pk_bf16_f32 v67, v192, v193\n"
        "ds_write_b64 v11, v[66:67] offset:1024\n"
        "s_setprio 0\n"
        "s_cmp_lg_u32 s47, 0\n"
        "s_cbranch_scc1 L%=_bhelper\n"
        "s_waitcnt vmcnt(5) lgkmcnt(0)\n"
        "s_barrier\n"
        "s_setprio 1\n"
        "ds_read_b64_tr_b16 v[176:177], v238 offset:16384\n"
        "ds_read_b128 v[32:35], v242 offset:0\n"
        "ds_read_b64_tr_b16 v[178:179], v238 offset:16896\n"
        "ds_read_b64_tr_b16 v[180:181], v239 offset:16384\n"
        "ds_read_b128 v[104:107], v242 offset:1024\n"
        "ds_read_b64_tr_b16 v[182:183], v239 offset:16896\n"
        "ds_read_b64_tr_b16 v[184:185], v238 offset:20480\n"
        "ds_read_b128 v[36:39], v243 offset:4096\n"
        "ds_read_b64_tr_b16 v[186:187], v238 offset:20992\n"
        "ds_read_b64_tr_b16 v[188:189], v239 offset:20480\n"
        "ds_read_b128 v[108:111], v243 offset:5120\n"
        "ds_read_b64_tr_b16 v[190:191], v239 offset:20992\n"
        "ds_read_b64_tr_b16 v[192:193], v238 offset:24576\n"
        "ds_read_b128 v[40:43], v242 offset:8192\n"
        "ds_read_b64_tr_b16 v[194:195], v238 offset:25088\n"
        "ds_read_b64_tr_b16 v[196:197], v239 offset:24576\n"
        "ds_read_b128 v[112:115], v242 offset:9216\n"
        "ds_read_b64_tr_b16 v[198:199], v239 offset:25088\n"
        "s_waitcnt lgkmcnt(12)\n"
        "v_mfma_f32_16x16x32_bf16 v[212:215], v[176:179], v[32:35], 0\n"
        "ds_read_b64_tr_b16 v[252:253], v238 offset:28672\n"
        "ds_read_b128 v[44:47], v243 offset:12288\n"
        "v_mfma_f32_16x16x32_bf16 v[216:219], v[180:183], v[32:35], 0\n"
        "ds_read_b64_tr_b16 v[254:255], v238 offset:29184\n"
        "v_mfma_f32_16x16x32_bf16 v[220:223], v[176:179], v[104:107], 0\n"
        "ds_read_b64_tr_b16 v[2:3], v239 offset:28672\n"
        "ds_read_b128 v[116:119], v243 offset:13312\n"
        "v_mfma_f32_16x16x32_bf16 v[224:227], v[180:183], v[104:107], 0\n"
        "ds_read_b64_tr_b16 v[4:5], v239 offset:29184\n"
        "s_waitcnt lgkmcnt(12)\n"
        "v_mfma_f32_16x16x32_bf16 v[212:215], v[184:187], v[36:39], v[212:215]\n"
        "ds_read_b64_tr_b16 v[176:177], v238 offset:32768\n"
        "ds_read_b128 v[48:51], v242 offset:16384\n"
        "v_mfma_f32_16x16x32_bf16 v[216:219], v[188:191], v[36:39], v[216:219]\n"
        "ds_read_b64_tr_b16 v[178:179], v238 offset:33280\n"
        "v_mfma_f32_16x16x32_bf16 v[220:223], v[184:187], v[108:111], v[220:223]\n"
        "ds_read_b64_tr_b16 v[180:181], v239 offset:32768\n"
        "ds_read_b128 v[120:123], v242 offset:17408\n"
        "v_mfma_f32_16x16x32_bf16 v[224:227], v[188:191], v[108:111], v[224:227]\n"
        "ds_read_b64_tr_b16 v[182:183], v239 offset:33280\n"
        "s_waitcnt lgkmcnt(12)\n"
        "v_mfma_f32_16x16x32_bf16 v[212:215], v[192:195], v[40:43], v[212:215]\n"
        "ds_read_b64_tr_b16 v[184:185], v238 offset:36864\n"
        "ds_read_b128 v[52:55], v243 offset:20480\n"
        "v_mfma_f32_16x16x32_bf16 v[216:219], v[196:199], v[40:43], v[216:219]\n"
        "ds_read_b64_tr_b16 v[186:187], v238 offset:37376\n"
        "v_mfma_f32_16x16x32_bf16 v[220:223], v[192:195], v[112:115], v[220:223]\n"
        "ds_read_b64_tr_b16 v[188:189], v239 offset:36864\n"
        "ds_read_b128 v[124:127], v243 offset:21504\n"
        "v_mfma_f32_16x16x32_bf16 v[224:227], v[196:199], v[112:115], v[224:227]\n"
        "ds_read_b64_tr_b16 v[190:191], v239 offset:37376\n"
        "s_waitcnt lgkmcnt(12)\n"
        "v_mfma_f32_16x16x32_bf16 v[212:215], v[252:255], v[44:47], v[212:215]\n"
        "ds_read_b64_tr_b16 v[192:193], v238 offset:40960\n"
        "ds_read_b128 v[56:59], v242 offset:24576\n"
        "v_mfma_f32_16x16x32_bf16 v[216:219], v[2:5], v[44:47], v[216:219]\n"
        "ds_read_b64_tr_b16 v[194:195], v238 offset:41472\n"
        "v_mfma_f32_16x16x32_bf16 v[220:223], v[252:255], v[116:119], v[220:223]\n"
        "ds_read_b64_tr_b16 v[196:197], v239 offset:40960\n"
        "ds_read_b128 v[128:131], v242 offset:25600\n"
        "v_mfma_f32_16x16x32_bf16 v[224:227], v[2:5], v[116:119], v[224:227]\n"
        "ds_read_b64_tr_b16 v[198:199], v239 offset:41472\n"
        "s_waitcnt vmcnt(3) lgkmcnt(12)\n"
        "s_barrier\n"
        "v_mfma_f32_16x16x32_bf16 v[212:215], v[176:179], v[48:51], v[212:215]\n"
        "ds_read_b64_tr_b16 v[252:253], v238 offset:45056\n"
        "ds_read_b128 v[60:63], v243 offset:28672\n"
        "v_mfma_f32_16x16x32_bf16 v[216:219], v[180:183], v[48:51], v[216:219]\n"
        "ds_read_b64_tr_b16 v[254:255], v238 offset:45568\n"
        "v_mfma_f32_16x16x32_bf16 v[220:223], v[176:179], v[120:123], v[220:223]\n"
        "ds_read_b64_tr_b16 v[2:3], v239 offset:45056\n"
        "ds_read_b128 v[132:135], v243 offset:29696\n"
        "v_mfma_f32_16x16x32_bf16 v[224:227], v[180:183], v[120:123], v[224:227]\n"
        "ds_read_b64_tr_b16 v[4:5], v239 offset:45568\n"
        "s_waitcnt lgkmcnt(12)\n"
        "v_mfma_f32_16x16x32_bf16 v[212:215], v[184:187], v[52:55], v[212:215]\n"
        "ds_read_b64_tr_b16 v[176:177], v238 offset:49152\n"
        "ds_read_b128 v[64:67], v242 offset:32768\n"
        "v_mfma_f32_16x16x32_bf16 v[216:219], v[188:191], v[52:55], v[216:219]\n"
        "ds_read_b64_tr_b16 v[178:179], v238 offset:49664\n"
        "v_mfma_f32_16x16x32_bf16 v[220:223], v[184:187], v[124:127], v[220:223]\n"
        "ds_read_b64_tr_b16 v[180:181], v239 offset:49152\n"
        "ds_read_b128 v[136:139], v242 offset:33792\n"
        "v_mfma_f32_16x16x32_bf16 v[224:227], v[188:191], v[124:127], v[224:227]\n"
        "ds_read_b64_tr_b16 v[182:183], v239 offset:49664\n"
        "s_waitcnt lgkmcnt(12)\n"
        "v_mfma_f32_16x16x32_bf16 v[212:215], v[192:195], v[56:59], v[212:215]\n"
        "ds_read_b64_tr_b16 v[184:185], v238 offset:53248\n"
        "ds_read_b128 v[68:71], v243 offset:36864\n"
        "v_mfma_f32_16x16x32_bf16 v[216:219], v[196:199], v[56:59], v[216:219]\n"
        "ds_read_b64_tr_b16 v[186:187], v238 offset:53760\n"
        "v_mfma_f32_16x16x32_bf16 v[220:223], v[192:195], v[128:131], v[220:223]\n"
        "ds_read_b64_tr_b16 v[188:189], v239 offset:53248\n"
        "ds_read_b128 v[140:143], v243 offset:37888\n"
        "v_mfma_f32_16x16x32_bf16 v[224:227], v[196:199], v[128:131], v[224:227]\n"
        "ds_read_b64_tr_b16 v[190:191], v239 offset:53760\n"
        "s_waitcnt lgkmcnt(12)\n"
        "v_mfma_f32_16x16x32_bf16 v[212:215], v[252:255], v[60:63], v[212:215]\n"
        "ds_read_b64_tr_b16 v[192:193], v238 offset:57344\n"
        "ds_read_b128 v[72:75], v242 offset:40960\n"
        "v_mfma_f32_16x16x32_bf16 v[216:219], v[2:5], v[60:63], v[216:219]\n"
        "ds_read_b64_tr_b16 v[194:195], v238 offset:57856\n"
        "v_mfma_f32_16x16x32_bf16 v[220:223], v[252:255], v[132:135], v[220:223]\n"
        "ds_read_b64_tr_b16 v[196:197], v239 offset:57344\n"
        "ds_read_b128 v[144:147], v242 offset:41984\n"
        "v_mfma_f32_16x16x32_bf16 v[224:227], v[2:5], v[132:135], v[224:227]\n"
        "ds_read_b64_tr_b16 v[198:199], v239 offset:57856\n"
        "s_waitcnt lgkmcnt(12)\n"
        "v_mfma_f32_16x16x32_bf16 v[212:215], v[176:179], v[64:67], v[212:215]\n"
        "ds_read_b64_tr_b16 v[252:253], v238 offset:61440\n"
        "ds_read_b128 v[76:79], v243 offset:45056\n"
        "v_mfma_f32_16x16x32_bf16 v[216:219], v[180:183], v[64:67], v[216:219]\n"
        "ds_read_b64_tr_b16 v[254:255], v238 offset:61952\n"
        "v_mfma_f32_16x16x32_bf16 v[220:223], v[176:179], v[136:139], v[220:223]\n"
        "ds_read_b64_tr_b16 v[2:3], v239 offset:61440\n"
        "ds_read_b128 v[148:151], v243 offset:46080\n"
        "v_mfma_f32_16x16x32_bf16 v[224:227], v[180:183], v[136:139], v[224:227]\n"
        "ds_read_b64_tr_b16 v[4:5], v239 offset:61952\n"
        "s_waitcnt vmcnt(0) lgkmcnt(12)\n"
        "s_barrier\n"
        "v_mfma_f32_16x16x32_bf16 v[212:215], v[184:187], v[68:71], v[212:215]\n"
        "ds_read_b64_tr_b16 v[176:177], v240 offset:0\n"
        "ds_read_b128 v[80:83], v242 offset:49152\n"
        "v_mfma_f32_16x16x32_bf16 v[216:219], v[188:191], v[68:71], v[216:219]\n"
        "ds_read_b64_tr_b16 v[178:179], v240 offset:512\n"
        "v_mfma_f32_16x16x32_bf16 v[220:223], v[184:187], v[140:143], v[220:223]\n"
        "ds_read_b64_tr_b16 v[180:181], v241 offset:0\n"
        "ds_read_b128 v[152:155], v242 offset:50176\n"
        "v_mfma_f32_16x16x32_bf16 v[224:227], v[188:191], v[140:143], v[224:227]\n"
        "ds_read_b64_tr_b16 v[182:183], v241 offset:512\n"
        "s_waitcnt lgkmcnt(12)\n"
        "v_mfma_f32_16x16x32_bf16 v[212:215], v[192:195], v[72:75], v[212:215]\n"
        "ds_read_b64_tr_b16 v[184:185], v240 offset:4096\n"
        "ds_read_b128 v[84:87], v243 offset:53248\n"
        "v_mfma_f32_16x16x32_bf16 v[216:219], v[196:199], v[72:75], v[216:219]\n"
        "ds_read_b64_tr_b16 v[186:187], v240 offset:4608\n"
        "v_mfma_f32_16x16x32_bf16 v[220:223], v[192:195], v[144:147], v[220:223]\n"
        "ds_read_b64_tr_b16 v[188:189], v241 offset:4096\n"
        "ds_read_b128 v[156:159], v243 offset:54272\n"
        "v_mfma_f32_16x16x32_bf16 v[224:227], v[196:199], v[144:147], v[224:227]\n"
        "ds_read_b64_tr_b16 v[190:191], v241 offset:4608\n"
        "s_waitcnt lgkmcnt(12)\n"
        "v_mfma_f32_16x16x32_bf16 v[212:215], v[252:255], v[76:79], v[212:215]\n"
        "ds_read_b64_tr_b16 v[192:193], v240 offset:8192\n"
        "ds_read_b128 v[88:91], v242 offset:57344\n"
        "v_mfma_f32_16x16x32_bf16 v[216:219], v[2:5], v[76:79], v[216:219]\n"
        "ds_read_b64_tr_b16 v[194:195], v240 offset:8704\n"
        "v_mfma_f32_16x16x32_bf16 v[220:223], v[252:255], v[148:151], v[220:223]\n"
        "ds_read_b64_tr_b16 v[196:197], v241 offset:8192\n"
        "ds_read_b128 v[160:163], v242 offset:58368\n"
        "v_mfma_f32_16x16x32_bf16 v[224:227], v[2:5], v[148:151], v[224:227]\n"
        "ds_read_b64_tr_b16 v[198:199], v241 offset:8704\n"
        "s_waitcnt lgkmcnt(12)\n"
        "v_mfma_f32_16x16x32_bf16 v[212:215], v[176:179], v[80:83], v[212:215]\n"
        "ds_read_b64_tr_b16 v[252:253], v240 offset:12288\n"
        "ds_read_b128 v[92:95], v243 offset:61440\n"
        "v_mfma_f32_16x16x32_bf16 v[216:219], v[180:183], v[80:83], v[216:219]\n"
        "ds_read_b64_tr_b16 v[254:255], v240 offset:12800\n"
        "v_mfma_f32_16x16x32_bf16 v[220:223], v[176:179], v[152:155], v[220:223]\n"
        "ds_read_b64_tr_b16 v[2:3], v241 offset:12288\n"
        "ds_read_b128 v[164:167], v243 offset:62464\n"
        "v_mfma_f32_16x16x32_bf16 v[224:227], v[180:183], v[152:155], v[224:227]\n"
        "ds_read_b64_tr_b16 v[4:5], v241 offset:12800\n"
        "s_waitcnt lgkmcnt(12)\n"
        "v_mfma_f32_16x16x32_bf16 v[212:215], v[184:187], v[84:87], v[212:215]\n"
        "ds_read_b64_tr_b16 v[176:177], v240 offset:16384\n"
        "ds_read_b128 v[96:99], v244 offset:0\n"
        "v_mfma_f32_16x16x32_bf16 v[216:219], v[188:191], v[84:87], v[216:219]\n"
        "ds_read_b64_tr_b16 v[178:179], v240 offset:16896\n"
        "v_mfma_f32_16x16x32_bf16 v[220:223], v[184:187], v[156:159], v[220:223]\n"
        "ds_read_b64_tr_b16 v[180:181], v241 offset:16384\n"
        "ds_read_b128 v[168:171], v244 offset:1024\n"
        "v_mfma_f32_16x16x32_bf16 v[224:227], v[188:191], v[156:159], v[224:227]\n"
        "ds_read_b64_tr_b16 v[182:183], v241 offset:16896\n"
        "s_waitcnt lgkmcnt(12)\n"
        "v_mfma_f32_16x16x32_bf16 v[212:215], v[192:195], v[88:91], v[212:215]\n"
        "ds_read_b64_tr_b16 v[184:185], v240 offset:20480\n"
        "ds_read_b128 v[100:103], v245 offset:4096\n"
        "v_mfma_f32_16x16x32_bf16 v[216:219], v[196:199], v[88:91], v[216:219]\n"
        "ds_read_b64_tr_b16 v[186:187], v240 offset:20992\n"
        "v_mfma_f32_16x16x32_bf16 v[220:223], v[192:195], v[160:163], v[220:223]\n"
        "ds_read_b64_tr_b16 v[188:189], v241 offset:20480\n"
        "ds_read_b128 v[172:175], v245 offset:5120\n"
        "v_mfma_f32_16x16x32_bf16 v[224:227], v[196:199], v[160:163], v[224:227]\n"
        "ds_read_b64_tr_b16 v[190:191], v241 offset:20992\n"
        "s_waitcnt lgkmcnt(12)\n"
        "s_barrier\n"
        "v_mfma_f32_16x16x32_bf16 v[212:215], v[252:255], v[92:95], v[212:215]\n"
        "ds_read_b64_tr_b16 v[192:193], v240 offset:28672\n"
        "v_mfma_f32_16x16x32_bf16 v[216:219], v[2:5], v[92:95], v[216:219]\n"
        "ds_read_b64_tr_b16 v[194:195], v240 offset:29184\n"
        "v_mfma_f32_16x16x32_bf16 v[220:223], v[252:255], v[164:167], v[220:223]\n"
        "ds_read_b64_tr_b16 v[196:197], v241 offset:28672\n"
        "v_mfma_f32_16x16x32_bf16 v[224:227], v[2:5], v[164:167], v[224:227]\n"
        "ds_read_b64_tr_b16 v[198:199], v241 offset:29184\n"
        "s_waitcnt lgkmcnt(10)\n"
        "v_mfma_f32_16x16x32_bf16 v[212:215], v[176:179], v[96:99], v[212:215]\n"
        "ds_read_b64_tr_b16 v[252:253], v240 offset:24576\n"
        "v_mfma_f32_16x16x32_bf16 v[216:219], v[180:183], v[96:99], v[216:219]\n"
        "ds_read_b64_tr_b16 v[254:255], v240 offset:25088\n"
        "v_mfma_f32_16x16x32_bf16 v[220:223], v[176:179], v[168:171], v[220:223]\n"
        "ds_read_b64_tr_b16 v[2:3], v241 offset:24576\n"
        "v_mfma_f32_16x16x32_bf16 v[224:227], v[180:183], v[168:171], v[224:227]\n"
        "ds_read_b64_tr_b16 v[4:5], v241 offset:25088\n"
        "s_waitcnt lgkmcnt(8)\n"
        "v_mfma_f32_16x16x32_bf16 v[212:215], v[184:187], v[100:103], v[212:215]\n"
        "ds_read_b64_tr_b16 v[176:177], v238 offset:12288\n"
        "v_mfma_f32_16x16x32_bf16 v[216:219], v[188:191], v[100:103], v[216:219]\n"
        "ds_read_b64_tr_b16 v[178:179], v238 offset:12800\n"
        "v_mfma_f32_16x16x32_bf16 v[220:223], v[184:187], v[172:175], v[220:223]\n"
        "ds_read_b64_tr_b16 v[180:181], v239 offset:12288\n"
        "v_mfma_f32_16x16x32_bf16 v[224:227], v[188:191], v[172:175], v[224:227]\n"
        "ds_read_b64_tr_b16 v[182:183], v239 offset:12800\n"
        "s_waitcnt lgkmcnt(8)\n"
        "v_cvt_pk_bf16_f32 v6, v212, v213\n"
        "v_cvt_pk_bf16_f32 v7, v214, v215\n"
        "v_mfma_f32_16x16x32_bf16 v[212:215], v[192:195], v[32:35], 0\n"
        "ds_read_b64_tr_b16 v[184:185], v238 offset:8192\n"
        "v_cvt_pk_bf16_f32 v8, v216, v217\n"
        "v_cvt_pk_bf16_f32 v9, v218, v219\n"
        "v_mfma_f32_16x16x32_bf16 v[216:219], v[196:199], v[32:35], 0\n"
        "ds_read_b64_tr_b16 v[186:187], v238 offset:8704\n"
        "v_cvt_pk_bf16_f32 v10, v220, v221\n"
        "v_cvt_pk_bf16_f32 v11, v222, v223\n"
        "v_mfma_f32_16x16x32_bf16 v[220:223], v[192:195], v[104:107], 0\n"
        "ds_read_b64_tr_b16 v[188:189], v239 offset:8192\n"
        "v_cvt_pk_bf16_f32 v12, v224, v225\n"
        "v_cvt_pk_bf16_f32 v13, v226, v227\n"
        "v_mfma_f32_16x16x32_bf16 v[224:227], v[196:199], v[104:107], 0\n"
        "ds_read_b64_tr_b16 v[190:191], v239 offset:8704\n"
        "s_waitcnt lgkmcnt(8)\n"
        "v_mfma_f32_16x16x32_bf16 v[212:215], v[252:255], v[36:39], v[212:215]\n"
        "ds_read_b64_tr_b16 v[192:193], v238 offset:32768\n"
        "v_mfma_f32_16x16x32_bf16 v[216:219], v[2:5], v[36:39], v[216:219]\n"
        "ds_read_b64_tr_b16 v[194:195], v238 offset:33280\n"
        "v_mfma_f32_16x16x32_bf16 v[220:223], v[252:255], v[108:111], v[220:223]\n"
        "ds_read_b64_tr_b16 v[196:197], v239 offset:32768\n"
        "v_mfma_f32_16x16x32_bf16 v[224:227], v[2:5], v[108:111], v[224:227]\n"
        "ds_read_b64_tr_b16 v[198:199], v239 offset:33280\n"
        "s_waitcnt lgkmcnt(8)\n"
        "v_mfma_f32_16x16x32_bf16 v[212:215], v[176:179], v[40:43], v[212:215]\n"
        "ds_read_b64_tr_b16 v[252:253], v238 offset:28672\n"
        "v_mfma_f32_16x16x32_bf16 v[216:219], v[180:183], v[40:43], v[216:219]\n"
        "ds_read_b64_tr_b16 v[254:255], v238 offset:29184\n"
        "v_mfma_f32_16x16x32_bf16 v[220:223], v[176:179], v[112:115], v[220:223]\n"
        "ds_read_b64_tr_b16 v[2:3], v239 offset:28672\n"
        "v_mfma_f32_16x16x32_bf16 v[224:227], v[180:183], v[112:115], v[224:227]\n"
        "ds_read_b64_tr_b16 v[4:5], v239 offset:29184\n"
        "s_waitcnt lgkmcnt(8)\n"
        "v_mfma_f32_16x16x32_bf16 v[212:215], v[184:187], v[44:47], v[212:215]\n"
        "ds_read_b64_tr_b16 v[176:177], v238 offset:24576\n"
        "v_mfma_f32_16x16x32_bf16 v[216:219], v[188:191], v[44:47], v[216:219]\n"
        "ds_read_b64_tr_b16 v[178:179], v238 offset:25088\n"
        "v_mfma_f32_16x16x32_bf16 v[220:223], v[184:187], v[116:119], v[220:223]\n"
        "ds_read_b64_tr_b16 v[180:181], v239 offset:24576\n"
        "v_mfma_f32_16x16x32_bf16 v[224:227], v[188:191], v[116:119], v[224:227]\n"
        "ds_read_b64_tr_b16 v[182:183], v239 offset:25088\n"
        "s_waitcnt lgkmcnt(8)\n"
        "v_mfma_f32_16x16x32_bf16 v[212:215], v[192:195], v[48:51], v[212:215]\n"
        "ds_read_b64_tr_b16 v[184:185], v238 offset:20480\n"
        "v_mfma_f32_16x16x32_bf16 v[216:219], v[196:199], v[48:51], v[216:219]\n"
        "ds_read_b64_tr_b16 v[186:187], v238 offset:20992\n"
        "v_mfma_f32_16x16x32_bf16 v[220:223], v[192:195], v[120:123], v[220:223]\n"
        "ds_read_b64_tr_b16 v[188:189], v239 offset:20480\n"
        "v_mfma_f32_16x16x32_bf16 v[224:227], v[196:199], v[120:123], v[224:227]\n"
        "ds_read_b64_tr_b16 v[190:191], v239 offset:20992\n"
        "s_waitcnt lgkmcnt(8)\n"
        "v_mfma_f32_16x16x32_bf16 v[212:215], v[252:255], v[52:55], v[212:215]\n"
        "ds_read_b64_tr_b16 v[192:193], v238 offset:16384\n"
        "v_mfma_f32_16x16x32_bf16 v[216:219], v[2:5], v[52:55], v[216:219]\n"
        "ds_read_b64_tr_b16 v[194:195], v238 offset:16896\n"
        "v_mfma_f32_16x16x32_bf16 v[220:223], v[252:255], v[124:127], v[220:223]\n"
        "ds_read_b64_tr_b16 v[196:197], v239 offset:16384\n"
        "v_mfma_f32_16x16x32_bf16 v[224:227], v[2:5], v[124:127], v[224:227]\n"
        "ds_read_b64_tr_b16 v[198:199], v239 offset:16896\n"
        "s_waitcnt lgkmcnt(8)\n"
        "s_barrier\n"
        "v_mfma_f32_16x16x32_bf16 v[212:215], v[176:179], v[56:59], v[212:215]\n"
        "ds_read_b64_tr_b16 v[252:253], v238 offset:53248\n"
        "v_mfma_f32_16x16x32_bf16 v[216:219], v[180:183], v[56:59], v[216:219]\n"
        "ds_read_b64_tr_b16 v[254:255], v238 offset:53760\n"
        "v_mfma_f32_16x16x32_bf16 v[220:223], v[176:179], v[128:131], v[220:223]\n"
        "ds_read_b64_tr_b16 v[2:3], v239 offset:53248\n"
        "v_mfma_f32_16x16x32_bf16 v[224:227], v[180:183], v[128:131], v[224:227]\n"
        "ds_read_b64_tr_b16 v[4:5], v239 offset:53760\n"
        "s_waitcnt lgkmcnt(8)\n"
        "v_mfma_f32_16x16x32_bf16 v[212:215], v[184:187], v[60:63], v[212:215]\n"
        "ds_read_b64_tr_b16 v[176:177], v238 offset:49152\n"
        "v_mfma_f32_16x16x32_bf16 v[216:219], v[188:191], v[60:63], v[216:219]\n"
        "ds_read_b64_tr_b16 v[178:179], v238 offset:49664\n"
        "v_mfma_f32_16x16x32_bf16 v[220:223], v[184:187], v[132:135], v[220:223]\n"
        "ds_read_b64_tr_b16 v[180:181], v239 offset:49152\n"
        "v_mfma_f32_16x16x32_bf16 v[224:227], v[188:191], v[132:135], v[224:227]\n"
        "ds_read_b64_tr_b16 v[182:183], v239 offset:49664\n"
        "s_waitcnt lgkmcnt(8)\n"
        "v_mfma_f32_16x16x32_bf16 v[212:215], v[192:195], v[64:67], v[212:215]\n"
        "ds_read_b64_tr_b16 v[184:185], v238 offset:45056\n"
        "v_mfma_f32_16x16x32_bf16 v[216:219], v[196:199], v[64:67], v[216:219]\n"
        "ds_read_b64_tr_b16 v[186:187], v238 offset:45568\n"
        "v_mfma_f32_16x16x32_bf16 v[220:223], v[192:195], v[136:139], v[220:223]\n"
        "ds_read_b64_tr_b16 v[188:189], v239 offset:45056\n"
        "v_mfma_f32_16x16x32_bf16 v[224:227], v[196:199], v[136:139], v[224:227]\n"
        "ds_read_b64_tr_b16 v[190:191], v239 offset:45568\n"
        "s_waitcnt lgkmcnt(8)\n"
        "v_mfma_f32_16x16x32_bf16 v[212:215], v[252:255], v[68:71], v[212:215]\n"
        "ds_read_b64_tr_b16 v[192:193], v238 offset:40960\n"
        "v_mfma_f32_16x16x32_bf16 v[216:219], v[2:5], v[68:71], v[216:219]\n"
        "ds_read_b64_tr_b16 v[194:195], v238 offset:41472\n"
        "v_mfma_f32_16x16x32_bf16 v[220:223], v[252:255], v[140:143], v[220:223]\n"
        "ds_read_b64_tr_b16 v[196:197], v239 offset:40960\n"
        "v_mfma_f32_16x16x32_bf16 v[224:227], v[2:5], v[140:143], v[224:227]\n"
        "ds_read_b64_tr_b16 v[198:199], v239 offset:41472\n"
        "s_waitcnt lgkmcnt(8)\n"
        "v_mfma_f32_16x16x32_bf16 v[212:215], v[176:179], v[72:75], v[212:215]\n"
        "ds_read_b64_tr_b16 v[252:253], v238 offset:36864\n"
        "v_mfma_f32_16x16x32_bf16 v[216:219], v[180:183], v[72:75], v[216:219]\n"
        "ds_read_b64_tr_b16 v[254:255], v238 offset:37376\n"
        "v_mfma_f32_16x16x32_bf16 v[220:223], v[176:179], v[144:147], v[220:223]\n"
        "ds_read_b64_tr_b16 v[2:3], v239 offset:36864\n"
        "v_mfma_f32_16x16x32_bf16 v[224:227], v[180:183], v[144:147], v[224:227]\n"
        "ds_read_b64_tr_b16 v[4:5], v239 offset:37376\n"
        "s_waitcnt lgkmcnt(8)\n"
        "v_mfma_f32_16x16x32_bf16 v[212:215], v[184:187], v[76:79], v[212:215]\n"
        "ds_read_b64_tr_b16 v[176:177], v236 offset:36864\n"
        "v_mfma_f32_16x16x32_bf16 v[216:219], v[188:191], v[76:79], v[216:219]\n"
        "ds_read_b64_tr_b16 v[178:179], v236 offset:37376\n"
        "v_mfma_f32_16x16x32_bf16 v[220:223], v[184:187], v[148:151], v[220:223]\n"
        "ds_read_b64_tr_b16 v[180:181], v237 offset:36864\n"
        "v_mfma_f32_16x16x32_bf16 v[224:227], v[188:191], v[148:151], v[224:227]\n"
        "ds_read_b64_tr_b16 v[182:183], v237 offset:37376\n"
        "s_waitcnt lgkmcnt(8)\n"
        "v_mfma_f32_16x16x32_bf16 v[212:215], v[192:195], v[80:83], v[212:215]\n"
        "ds_read_b64_tr_b16 v[184:185], v236 offset:32768\n"
        "v_mfma_f32_16x16x32_bf16 v[216:219], v[196:199], v[80:83], v[216:219]\n"
        "ds_read_b64_tr_b16 v[186:187], v236 offset:33280\n"
        "v_mfma_f32_16x16x32_bf16 v[220:223], v[192:195], v[152:155], v[220:223]\n"
        "ds_read_b64_tr_b16 v[188:189], v237 offset:32768\n"
        "v_mfma_f32_16x16x32_bf16 v[224:227], v[196:199], v[152:155], v[224:227]\n"
        "ds_read_b64_tr_b16 v[190:191], v237 offset:33280\n"
        "s_waitcnt lgkmcnt(8)\n"
        "v_mfma_f32_16x16x32_bf16 v[212:215], v[252:255], v[84:87], v[212:215]\n"
        "ds_read_b64_tr_b16 v[192:193], v236 offset:28672\n"
        "v_mfma_f32_16x16x32_bf16 v[216:219], v[2:5], v[84:87], v[216:219]\n"
        "ds_read_b64_tr_b16 v[194:195], v236 offset:29184\n"
        "v_mfma_f32_16x16x32_bf16 v[220:223], v[252:255], v[156:159], v[220:223]\n"
        "ds_read_b64_tr_b16 v[196:197], v237 offset:28672\n"
        "v_mfma_f32_16x16x32_bf16 v[224:227], v[2:5], v[156:159], v[224:227]\n"
        "ds_read_b64_tr_b16 v[198:199], v237 offset:29184\n"
        "s_waitcnt lgkmcnt(8)\n"
        "v_mfma_f32_16x16x32_bf16 v[212:215], v[176:179], v[88:91], v[212:215]\n"
        "ds_read_b64_tr_b16 v[252:253], v236 offset:24576\n"
        "v_mfma_f32_16x16x32_bf16 v[216:219], v[180:183], v[88:91], v[216:219]\n"
        "ds_read_b64_tr_b16 v[254:255], v236 offset:25088\n"
        "v_mfma_f32_16x16x32_bf16 v[220:223], v[176:179], v[160:163], v[220:223]\n"
        "ds_read_b64_tr_b16 v[2:3], v237 offset:24576\n"
        "v_mfma_f32_16x16x32_bf16 v[224:227], v[180:183], v[160:163], v[224:227]\n"
        "ds_read_b64_tr_b16 v[4:5], v237 offset:25088\n"
        "s_waitcnt lgkmcnt(8)\n"
        "s_barrier\n"
        "v_mfma_f32_16x16x32_bf16 v[212:215], v[184:187], v[92:95], v[212:215]\n"
        "ds_read_b64_tr_b16 v[176:177], v240 offset:12288\n"
        "v_mfma_f32_16x16x32_bf16 v[216:219], v[188:191], v[92:95], v[216:219]\n"
        "ds_read_b64_tr_b16 v[178:179], v240 offset:12800\n"
        "v_mfma_f32_16x16x32_bf16 v[220:223], v[184:187], v[164:167], v[220:223]\n"
        "ds_read_b64_tr_b16 v[180:181], v241 offset:12288\n"
        "v_mfma_f32_16x16x32_bf16 v[224:227], v[188:191], v[164:167], v[224:227]\n"
        "ds_read_b64_tr_b16 v[182:183], v241 offset:12800\n"
        "s_waitcnt lgkmcnt(8)\n"
        "v_mfma_f32_16x16x32_bf16 v[212:215], v[192:195], v[96:99], v[212:215]\n"
        "ds_read_b64_tr_b16 v[184:185], v240 offset:8192\n"
        "v_mfma_f32_16x16x32_bf16 v[216:219], v[196:199], v[96:99], v[216:219]\n"
        "ds_read_b64_tr_b16 v[186:187], v240 offset:8704\n"
        "v_mfma_f32_16x16x32_bf16 v[220:223], v[192:195], v[168:171], v[220:223]\n"
        "ds_read_b64_tr_b16 v[188:189], v241 offset:8192\n"
        "v_mfma_f32_16x16x32_bf16 v[224:227], v[196:199], v[168:171], v[224:227]\n"
        "ds_read_b64_tr_b16 v[190:191], v241 offset:8704\n"
        "s_waitcnt lgkmcnt(8)\n"
        "v_mfma_f32_16x16x32_bf16 v[212:215], v[252:255], v[100:103], v[212:215]\n"
        "ds_read_b64_tr_b16 v[192:193], v240 offset:4096\n"
        "v_mfma_f32_16x16x32_bf16 v[216:219], v[2:5], v[100:103], v[216:219]\n"
        "ds_read_b64_tr_b16 v[194:195], v240 offset:4608\n"
        "v_mfma_f32_16x16x32_bf16 v[220:223], v[252:255], v[172:175], v[220:223]\n"
        "ds_read_b64_tr_b16 v[196:197], v241 offset:4096\n"
        "v_mfma_f32_16x16x32_bf16 v[224:227], v[2:5], v[172:175], v[224:227]\n"
        "ds_read_b64_tr_b16 v[198:199], v241 offset:4608\n"
        "s_waitcnt lgkmcnt(8)\n"
        "v_cvt_pk_bf16_f32 v14, v212, v213\n"
        "v_cvt_pk_bf16_f32 v15, v214, v215\n"
        "v_mfma_f32_16x16x32_bf16 v[212:215], v[176:179], v[32:35], 0\n"
        "ds_read_b64_tr_b16 v[252:253], v240 offset:0\n"
        "v_cvt_pk_bf16_f32 v16, v216, v217\n"
        "v_cvt_pk_bf16_f32 v17, v218, v219\n"
        "v_mfma_f32_16x16x32_bf16 v[216:219], v[180:183], v[32:35], 0\n"
        "ds_read_b64_tr_b16 v[254:255], v240 offset:512\n"
        "v_cvt_pk_bf16_f32 v18, v220, v221\n"
        "v_cvt_pk_bf16_f32 v19, v222, v223\n"
        "v_mfma_f32_16x16x32_bf16 v[220:223], v[176:179], v[104:107], 0\n"
        "ds_read_b64_tr_b16 v[2:3], v241 offset:0\n"
        "v_cvt_pk_bf16_f32 v20, v224, v225\n"
        "v_cvt_pk_bf16_f32 v21, v226, v227\n"
        "v_mfma_f32_16x16x32_bf16 v[224:227], v[180:183], v[104:107], 0\n"
        "ds_read_b64_tr_b16 v[4:5], v241 offset:512\n"
        "s_waitcnt lgkmcnt(8)\n"
        "v_mfma_f32_16x16x32_bf16 v[212:215], v[184:187], v[36:39], v[212:215]\n"
        "ds_read_b64_tr_b16 v[176:177], v238 offset:61440\n"
        "v_mfma_f32_16x16x32_bf16 v[216:219], v[188:191], v[36:39], v[216:219]\n"
        "ds_read_b64_tr_b16 v[178:179], v238 offset:61952\n"
        "v_mfma_f32_16x16x32_bf16 v[220:223], v[184:187], v[108:111], v[220:223]\n"
        "ds_read_b64_tr_b16 v[180:181], v239 offset:61440\n"
        "v_mfma_f32_16x16x32_bf16 v[224:227], v[188:191], v[108:111], v[224:227]\n"
        "ds_read_b64_tr_b16 v[182:183], v239 offset:61952\n"
        "s_waitcnt lgkmcnt(8)\n"
        "v_mfma_f32_16x16x32_bf16 v[212:215], v[192:195], v[40:43], v[212:215]\n"
        "ds_read_b64_tr_b16 v[184:185], v238 offset:57344\n"
        "v_mfma_f32_16x16x32_bf16 v[216:219], v[196:199], v[40:43], v[216:219]\n"
        "ds_read_b64_tr_b16 v[186:187], v238 offset:57856\n"
        "v_mfma_f32_16x16x32_bf16 v[220:223], v[192:195], v[112:115], v[220:223]\n"
        "ds_read_b64_tr_b16 v[188:189], v239 offset:57344\n"
        "v_mfma_f32_16x16x32_bf16 v[224:227], v[196:199], v[112:115], v[224:227]\n"
        "ds_read_b64_tr_b16 v[190:191], v239 offset:57856\n"
        "s_waitcnt lgkmcnt(8)\n"
        "v_mfma_f32_16x16x32_bf16 v[212:215], v[252:255], v[44:47], v[212:215]\n"
        "ds_read_b64_tr_b16 v[192:193], v236 offset:61440\n"
        "v_mfma_f32_16x16x32_bf16 v[216:219], v[2:5], v[44:47], v[216:219]\n"
        "ds_read_b64_tr_b16 v[194:195], v236 offset:61952\n"
        "v_mfma_f32_16x16x32_bf16 v[220:223], v[252:255], v[116:119], v[220:223]\n"
        "ds_read_b64_tr_b16 v[196:197], v237 offset:61440\n"
        "v_mfma_f32_16x16x32_bf16 v[224:227], v[2:5], v[116:119], v[224:227]\n"
        "ds_read_b64_tr_b16 v[198:199], v237 offset:61952\n"
        "s_waitcnt lgkmcnt(8)\n"
        "v_mfma_f32_16x16x32_bf16 v[212:215], v[176:179], v[48:51], v[212:215]\n"
        "ds_read_b64_tr_b16 v[252:253], v236 offset:57344\n"
        "v_mfma_f32_16x16x32_bf16 v[216:219], v[180:183], v[48:51], v[216:219]\n"
        "ds_read_b64_tr_b16 v[254:255], v236 offset:57856\n"
        "v_mfma_f32_16x16x32_bf16 v[220:223], v[176:179], v[120:123], v[220:223]\n"
        "ds_read_b64_tr_b16 v[2:3], v237 offset:57344\n"
        "v_mfma_f32_16x16x32_bf16 v[224:227], v[180:183], v[120:123], v[224:227]\n"
        "ds_read_b64_tr_b16 v[4:5], v237 offset:57856\n"
        "s_waitcnt lgkmcnt(8)\n"
        "v_mfma_f32_16x16x32_bf16 v[212:215], v[184:187], v[52:55], v[212:215]\n"
        "ds_read_b64_tr_b16 v[176:177], v236 offset:53248\n"
        "v_mfma_f32_16x16x32_bf16 v[216:219], v[188:191], v[52:55], v[216:219]\n"
        "ds_read_b64_tr_b16 v[178:179], v236 offset:53760\n"
        "v_mfma_f32_16x16x32_bf16 v[220:223], v[184:187], v[124:127], v[220:223]\n"
        "ds_read_b64_tr_b16 v[180:181], v237 offset:53248\n"
        "v_mfma_f32_16x16x32_bf16 v[224:227], v[188:191], v[124:127], v[224:227]\n"
        "ds_read_b64_tr_b16 v[182:183], v237 offset:53760\n"
        "s_waitcnt lgkmcnt(8)\n"
        "s_barrier\n"
        "v_mfma_f32_16x16x32_bf16 v[212:215], v[192:195], v[56:59], v[212:215]\n"
        "ds_read_b64_tr_b16 v[184:185], v236 offset:49152\n"
        "v_mfma_f32_16x16x32_bf16 v[216:219], v[196:199], v[56:59], v[216:219]\n"
        "ds_read_b64_tr_b16 v[186:187], v236 offset:49664\n"
        "v_mfma_f32_16x16x32_bf16 v[220:223], v[192:195], v[128:131], v[220:223]\n"
        "ds_read_b64_tr_b16 v[188:189], v237 offset:49152\n"
        "v_mfma_f32_16x16x32_bf16 v[224:227], v[196:199], v[128:131], v[224:227]\n"
        "ds_read_b64_tr_b16 v[190:191], v237 offset:49664\n"
        "s_waitcnt lgkmcnt(8)\n"
        "v_mfma_f32_16x16x32_bf16 v[212:215], v[252:255], v[60:63], v[212:215]\n"
        "ds_read_b64_tr_b16 v[192:193], v240 offset:28672\n"
        "v_mfma_f32_16x16x32_bf16 v[216:219], v[2:5], v[60:63], v[216:219]\n"
        "ds_read_b64_tr_b16 v[194:195], v240 offset:29184\n"
        "v_mfma_f32_16x16x32_bf16 v[220:223], v[252:255], v[132:135], v[220:223]\n"
        "ds_read_b64_tr_b16 v[196:197], v241 offset:28672\n"
        "v_mfma_f32_16x16x32_bf16 v[224:227], v[2:5], v[132:135], v[224:227]\n"
        "ds_read_b64_tr_b16 v[198:199], v241 offset:29184\n"
        "s_waitcnt lgkmcnt(8)\n"
        "v_mfma_f32_16x16x32_bf16 v[212:215], v[176:179], v[64:67], v[212:215]\n"
        "ds_read_b64_tr_b16 v[252:253], v240 offset:24576\n"
        "v_mfma_f32_16x16x32_bf16 v[216:219], v[180:183], v[64:67], v[216:219]\n"
        "ds_read_b64_tr_b16 v[254:255], v240 offset:25088\n"
        "v_mfma_f32_16x16x32_bf16 v[220:223], v[176:179], v[136:139], v[220:223]\n"
        "ds_read_b64_tr_b16 v[2:3], v241 offset:24576\n"
        "v_mfma_f32_16x16x32_bf16 v[224:227], v[180:183], v[136:139], v[224:227]\n"
        "ds_read_b64_tr_b16 v[4:5], v241 offset:25088\n"
        "s_waitcnt lgkmcnt(8)\n"
        "v_mfma_f32_16x16x32_bf16 v[212:215], v[184:187], v[68:71], v[212:215]\n"
        "ds_read_b64_tr_b16 v[176:177], v240 offset:20480\n"
        "v_mfma_f32_16x16x32_bf16 v[216:219], v[188:191], v[68:71], v[216:219]\n"
        "ds_read_b64_tr_b16 v[178:179], v240 offset:20992\n"
        "v_mfma_f32_16x16x32_bf16 v[220:223], v[184:187], v[140:143], v[220:223]\n"
        "ds_read_b64_tr_b16 v[180:181], v241 offset:20480\n"
        "v_mfma_f32_16x16x32_bf16 v[224:227], v[188:191], v[140:143], v[224:227]\n"
        "ds_read_b64_tr_b16 v[182:183], v241 offset:20992\n"
        "s_waitcnt lgkmcnt(8)\n"
        "v_mfma_f32_16x16x32_bf16 v[212:215], v[192:195], v[72:75], v[212:215]\n"
        "ds_read_b64_tr_b16 v[184:185], v240 offset:16384\n"
        "v_mfma_f32_16x16x32_bf16 v[216:219], v[196:199], v[72:75], v[216:219]\n"
        "ds_read_b64_tr_b16 v[186:187], v240 offset:16896\n"
        "v_mfma_f32_16x16x32_bf16 v[220:223], v[192:195], v[144:147], v[220:223]\n"
        "ds_read_b64_tr_b16 v[188:189], v241 offset:16384\n"
        "v_mfma_f32_16x16x32_bf16 v[224:227], v[196:199], v[144:147], v[224:227]\n"
        "ds_read_b64_tr_b16 v[190:191], v241 offset:16896\n"
        "s_waitcnt lgkmcnt(8)\n"
        "v_mfma_f32_16x16x32_bf16 v[212:215], v[252:255], v[76:79], v[212:215]\n"
        "ds_read_b64_tr_b16 v[192:193], v238 offset:32768\n"
        "v_mfma_f32_16x16x32_bf16 v[216:219], v[2:5], v[76:79], v[216:219]\n"
        "ds_read_b64_tr_b16 v[194:195], v238 offset:33280\n"
        "v_mfma_f32_16x16x32_bf16 v[220:223], v[252:255], v[148:151], v[220:223]\n"
        "ds_read_b64_tr_b16 v[196:197], v239 offset:32768\n"
        "v_mfma_f32_16x16x32_bf16 v[224:227], v[2:5], v[148:151], v[224:227]\n"
        "ds_read_b64_tr_b16 v[198:199], v239 offset:33280\n"
        "s_waitcnt lgkmcnt(8)\n"
        "v_mfma_f32_16x16x32_bf16 v[212:215], v[176:179], v[80:83], v[212:215]\n"
        "ds_read_b64_tr_b16 v[252:253], v238 offset:28672\n"
        "v_mfma_f32_16x16x32_bf16 v[216:219], v[180:183], v[80:83], v[216:219]\n"
        "ds_read_b64_tr_b16 v[254:255], v238 offset:29184\n"
        "v_mfma_f32_16x16x32_bf16 v[220:223], v[176:179], v[152:155], v[220:223]\n"
        "ds_read_b64_tr_b16 v[2:3], v239 offset:28672\n"
        "v_mfma_f32_16x16x32_bf16 v[224:227], v[180:183], v[152:155], v[224:227]\n"
        "ds_read_b64_tr_b16 v[4:5], v239 offset:29184\n"
        "s_waitcnt lgkmcnt(8)\n"
        "v_mfma_f32_16x16x32_bf16 v[212:215], v[184:187], v[84:87], v[212:215]\n"
        "ds_read_b64_tr_b16 v[176:177], v238 offset:24576\n"
        "v_mfma_f32_16x16x32_bf16 v[216:219], v[188:191], v[84:87], v[216:219]\n"
        "ds_read_b64_tr_b16 v[178:179], v238 offset:25088\n"
        "v_mfma_f32_16x16x32_bf16 v[220:223], v[184:187], v[156:159], v[220:223]\n"
        "ds_read_b64_tr_b16 v[180:181], v239 offset:24576\n"
        "v_mfma_f32_16x16x32_bf16 v[224:227], v[188:191], v[156:159], v[224:227]\n"
        "ds_read_b64_tr_b16 v[182:183], v239 offset:25088\n"
        "s_waitcnt lgkmcnt(8)\n"
        "v_mfma_f32_16x16x32_bf16 v[212:215], v[192:195], v[88:91], v[212:215]\n"
        "ds_read_b64_tr_b16 v[184:185], v238 offset:12288\n"
        "v_mfma_f32_16x16x32_bf16 v[216:219], v[196:199], v[88:91], v[216:219]\n"
        "ds_read_b64_tr_b16 v[186:187], v238 offset:12800\n"
        "v_mfma_f32_16x16x32_bf16 v[220:223], v[192:195], v[160:163], v[220:223]\n"
        "ds_read_b64_tr_b16 v[188:189], v239 offset:12288\n"
        "v_mfma_f32_16x16x32_bf16 v[224:227], v[196:199], v[160:163], v[224:227]\n"
        "ds_read_b64_tr_b16 v[190:191], v239 offset:12800\n"
        "s_waitcnt lgkmcnt(8)\n"
        "s_barrier\n"
        "v_mfma_f32_16x16x32_bf16 v[212:215], v[252:255], v[92:95], v[212:215]\n"
        "ds_read_b64_tr_b16 v[192:193], v238 offset:8192\n"
        "v_mfma_f32_16x16x32_bf16 v[216:219], v[2:5], v[92:95], v[216:219]\n"
        "ds_read_b64_tr_b16 v[194:195], v238 offset:8704\n"
        "v_mfma_f32_16x16x32_bf16 v[220:223], v[252:255], v[164:167], v[220:223]\n"
        "ds_read_b64_tr_b16 v[196:197], v239 offset:8192\n"
        "v_mfma_f32_16x16x32_bf16 v[224:227], v[2:5], v[164:167], v[224:227]\n"
        "ds_read_b64_tr_b16 v[198:199], v239 offset:8704\n"
        "s_waitcnt lgkmcnt(8)\n"
        "v_mfma_f32_16x16x32_bf16 v[212:215], v[176:179], v[96:99], v[212:215]\n"
        "ds_read_b64_tr_b16 v[252:253], v238 offset:4096\n"
        "v_mfma_f32_16x16x32_bf16 v[216:219], v[180:183], v[96:99], v[216:219]\n"
        "ds_read_b64_tr_b16 v[254:255], v238 offset:4608\n"
        "v_mfma_f32_16x16x32_bf16 v[220:223], v[176:179], v[168:171], v[220:223]\n"
        "ds_read_b64_tr_b16 v[2:3], v239 offset:4096\n"
        "v_mfma_f32_16x16x32_bf16 v[224:227], v[180:183], v[168:171], v[224:227]\n"
        "ds_read_b64_tr_b16 v[4:5], v239 offset:4608\n"
        "s_waitcnt lgkmcnt(8)\n"
        "v_mfma_f32_16x16x32_bf16 v[212:215], v[184:187], v[100:103], v[212:215]\n"
        "ds_read_b64_tr_b16 v[176:177], v238 offset:53248\n"
        "v_mfma_f32_16x16x32_bf16 v[216:219], v[188:191], v[100:103], v[216:219]\n"
        "ds_read_b64_tr_b16 v[178:179], v238 offset:53760\n"
        "v_mfma_f32_16x16x32_bf16 v[220:223], v[184:187], v[172:175], v[220:223]\n"
        "ds_read_b64_tr_b16 v[180:181], v239 offset:53248\n"
        "v_mfma_f32_16x16x32_bf16 v[224:227], v[188:191], v[172:175], v[224:227]\n"
        "ds_read_b64_tr_b16 v[182:183], v239 offset:53760\n"
        "s_waitcnt lgkmcnt(8)\n"
        "v_cvt_pk_bf16_f32 v22, v212, v213\n"
        "v_cvt_pk_bf16_f32 v23, v214, v215\n"
        "v_mfma_f32_16x16x32_bf16 v[212:215], v[192:195], v[32:35], 0\n"
        "ds_read_b64_tr_b16 v[184:185], v238 offset:49152\n"
        "v_cvt_pk_bf16_f32 v24, v216, v217\n"
        "v_cvt_pk_bf16_f32 v25, v218, v219\n"
        "v_mfma_f32_16x16x32_bf16 v[216:219], v[196:199], v[32:35], 0\n"
        "ds_read_b64_tr_b16 v[186:187], v238 offset:49664\n"
        "v_cvt_pk_bf16_f32 v26, v220, v221\n"
        "v_cvt_pk_bf16_f32 v27, v222, v223\n"
        "v_mfma_f32_16x16x32_bf16 v[220:223], v[192:195], v[104:107], 0\n"
        "ds_read_b64_tr_b16 v[188:189], v239 offset:49152\n"
        "v_cvt_pk_bf16_f32 v28, v224, v225\n"
        "v_cvt_pk_bf16_f32 v29, v226, v227\n"
        "v_mfma_f32_16x16x32_bf16 v[224:227], v[196:199], v[104:107], 0\n"
        "ds_read_b64_tr_b16 v[190:191], v239 offset:49664\n"
        "s_waitcnt lgkmcnt(8)\n"
        "v_mfma_f32_16x16x32_bf16 v[212:215], v[252:255], v[36:39], v[212:215]\n"
        "ds_read_b64_tr_b16 v[192:193], v238 offset:45056\n"
        "v_mfma_f32_16x16x32_bf16 v[216:219], v[2:5], v[36:39], v[216:219]\n"
        "ds_read_b64_tr_b16 v[194:195], v238 offset:45568\n"
        "v_mfma_f32_16x16x32_bf16 v[220:223], v[252:255], v[108:111], v[220:223]\n"
        "ds_read_b64_tr_b16 v[196:197], v239 offset:45056\n"
        "v_mfma_f32_16x16x32_bf16 v[224:227], v[2:5], v[108:111], v[224:227]\n"
        "ds_read_b64_tr_b16 v[198:199], v239 offset:45568\n"
        "s_waitcnt lgkmcnt(8)\n"
        "v_mfma_f32_16x16x32_bf16 v[212:215], v[176:179], v[40:43], v[212:215]\n"
        "ds_read_b64_tr_b16 v[252:253], v238 offset:40960\n"
        "v_mfma_f32_16x16x32_bf16 v[216:219], v[180:183], v[40:43], v[216:219]\n"
        "ds_read_b64_tr_b16 v[254:255], v238 offset:41472\n"
        "v_mfma_f32_16x16x32_bf16 v[220:223], v[176:179], v[112:115], v[220:223]\n"
        "ds_read_b64_tr_b16 v[2:3], v239 offset:40960\n"
        "v_mfma_f32_16x16x32_bf16 v[224:227], v[180:183], v[112:115], v[224:227]\n"
        "ds_read_b64_tr_b16 v[4:5], v239 offset:41472\n"
        "s_waitcnt lgkmcnt(8)\n"
        "v_mfma_f32_16x16x32_bf16 v[212:215], v[184:187], v[44:47], v[212:215]\n"
        "ds_read_b64_tr_b16 v[176:177], v238 offset:36864\n"
        "v_mfma_f32_16x16x32_bf16 v[216:219], v[188:191], v[44:47], v[216:219]\n"
        "ds_read_b64_tr_b16 v[178:179], v238 offset:37376\n"
        "v_mfma_f32_16x16x32_bf16 v[220:223], v[184:187], v[116:119], v[220:223]\n"
        "ds_read_b64_tr_b16 v[180:181], v239 offset:36864\n"
        "v_mfma_f32_16x16x32_bf16 v[224:227], v[188:191], v[116:119], v[224:227]\n"
        "ds_read_b64_tr_b16 v[182:183], v239 offset:37376\n"
        "s_waitcnt lgkmcnt(8)\n"
        "v_mfma_f32_16x16x32_bf16 v[212:215], v[192:195], v[48:51], v[212:215]\n"
        "ds_read_b64_tr_b16 v[184:185], v238 offset:20480\n"
        "v_mfma_f32_16x16x32_bf16 v[216:219], v[196:199], v[48:51], v[216:219]\n"
        "ds_read_b64_tr_b16 v[186:187], v238 offset:20992\n"
        "v_mfma_f32_16x16x32_bf16 v[220:223], v[192:195], v[120:123], v[220:223]\n"
        "ds_read_b64_tr_b16 v[188:189], v239 offset:20480\n"
        "v_mfma_f32_16x16x32_bf16 v[224:227], v[196:199], v[120:123], v[224:227]\n"
        "ds_read_b64_tr_b16 v[190:191], v239 offset:20992\n"
        "s_waitcnt lgkmcnt(8)\n"
        "v_mfma_f32_16x16x32_bf16 v[212:215], v[252:255], v[52:55], v[212:215]\n"
        "ds_read_b64_tr_b16 v[192:193], v238 offset:16384\n"
        "v_mfma_f32_16x16x32_bf16 v[216:219], v[2:5], v[52:55], v[216:219]\n"
        "ds_read_b64_tr_b16 v[194:195], v238 offset:16896\n"
        "v_mfma_f32_16x16x32_bf16 v[220:223], v[252:255], v[124:127], v[220:223]\n"
        "ds_read_b64_tr_b16 v[196:197], v239 offset:16384\n"
        "v_mfma_f32_16x16x32_bf16 v[224:227], v[2:5], v[124:127], v[224:227]\n"
        "ds_read_b64_tr_b16 v[198:199], v239 offset:16896\n"
        "s_waitcnt lgkmcnt(8)\n"
        "s_barrier\n"
        "v_mfma_f32_16x16x32_bf16 v[212:215], v[176:179], v[56:59], v[212:215]\n"
        "ds_read_b64_tr_b16 v[252:253], v238 offset:0\n"
        "v_mfma_f32_16x16x32_bf16 v[216:219], v[180:183], v[56:59], v[216:219]\n"
        "ds_read_b64_tr_b16 v[254:255], v238 offset:512\n"
        "v_mfma_f32_16x16x32_bf16 v[220:223], v[176:179], v[128:131], v[220:223]\n"
        "ds_read_b64_tr_b16 v[2:3], v239 offset:0\n"
        "v_mfma_f32_16x16x32_bf16 v[224:227], v[180:183], v[128:131], v[224:227]\n"
        "ds_read_b64_tr_b16 v[4:5], v239 offset:512\n"
        "s_waitcnt lgkmcnt(8)\n"
        "v_mfma_f32_16x16x32_bf16 v[212:215], v[184:187], v[60:63], v[212:215]\n"
        "ds_read_b64_tr_b16 v[176:177], v236 offset:45056\n"
        "v_mfma_f32_16x16x32_bf16 v[216:219], v[188:191], v[60:63], v[216:219]\n"
        "ds_read_b64_tr_b16 v[178:179], v236 offset:45568\n"
        "v_mfma_f32_16x16x32_bf16 v[220:223], v[184:187], v[132:135], v[220:223]\n"
        "ds_read_b64_tr_b16 v[180:181], v237 offset:45056\n"
        "v_mfma_f32_16x16x32_bf16 v[224:227], v[188:191], v[132:135], v[224:227]\n"
        "ds_read_b64_tr_b16 v[182:183], v237 offset:45568\n"
        "s_waitcnt lgkmcnt(8)\n"
        "v_mfma_f32_16x16x32_bf16 v[212:215], v[192:195], v[64:67], v[212:215]\n"
        "ds_read_b64_tr_b16 v[184:185], v236 offset:40960\n"
        "v_mfma_f32_16x16x32_bf16 v[216:219], v[196:199], v[64:67], v[216:219]\n"
        "ds_read_b64_tr_b16 v[186:187], v236 offset:41472\n"
        "v_mfma_f32_16x16x32_bf16 v[220:223], v[192:195], v[136:139], v[220:223]\n"
        "ds_read_b64_tr_b16 v[188:189], v237 offset:40960\n"
        "v_mfma_f32_16x16x32_bf16 v[224:227], v[196:199], v[136:139], v[224:227]\n"
        "ds_read_b64_tr_b16 v[190:191], v237 offset:41472\n"
        "s_waitcnt lgkmcnt(8)\n"
        "v_mfma_f32_16x16x32_bf16 v[212:215], v[252:255], v[68:71], v[212:215]\n"
        "ds_read_b64_tr_b16 v[192:193], v240 offset:12288\n"
        "v_mfma_f32_16x16x32_bf16 v[216:219], v[2:5], v[68:71], v[216:219]\n"
        "ds_read_b64_tr_b16 v[194:195], v240 offset:12800\n"
        "v_mfma_f32_16x16x32_bf16 v[220:223], v[252:255], v[140:143], v[220:223]\n"
        "ds_read_b64_tr_b16 v[196:197], v241 offset:12288\n"
        "v_mfma_f32_16x16x32_bf16 v[224:227], v[2:5], v[140:143], v[224:227]\n"
        "ds_read_b64_tr_b16 v[198:199], v241 offset:12800\n"
        "s_waitcnt lgkmcnt(8)\n"
        "v_mfma_f32_16x16x32_bf16 v[212:215], v[176:179], v[72:75], v[212:215]\n"
        "ds_read_b64_tr_b16 v[252:253], v240 offset:8192\n"
        "v_mfma_f32_16x16x32_bf16 v[216:219], v[180:183], v[72:75], v[216:219]\n"
        "ds_read_b64_tr_b16 v[254:255], v240 offset:8704\n"
        "v_mfma_f32_16x16x32_bf16 v[220:223], v[176:179], v[144:147], v[220:223]\n"
        "ds_read_b64_tr_b16 v[2:3], v241 offset:8192\n"
        "v_mfma_f32_16x16x32_bf16 v[224:227], v[180:183], v[144:147], v[224:227]\n"
        "ds_read_b64_tr_b16 v[4:5], v241 offset:8704\n"
        "s_waitcnt lgkmcnt(8)\n"
        "v_mfma_f32_16x16x32_bf16 v[212:215], v[184:187], v[76:79], v[212:215]\n"
        "ds_read_b64_tr_b16 v[176:177], v240 offset:4096\n"
        "v_mfma_f32_16x16x32_bf16 v[216:219], v[188:191], v[76:79], v[216:219]\n"
        "ds_read_b64_tr_b16 v[178:179], v240 offset:4608\n"
        "v_mfma_f32_16x16x32_bf16 v[220:223], v[184:187], v[148:151], v[220:223]\n"
        "ds_read_b64_tr_b16 v[180:181], v241 offset:4096\n"
        "v_mfma_f32_16x16x32_bf16 v[224:227], v[188:191], v[148:151], v[224:227]\n"
        "ds_read_b64_tr_b16 v[182:183], v241 offset:4608\n"
        "s_waitcnt lgkmcnt(8)\n"
        "v_mfma_f32_16x16x32_bf16 v[212:215], v[192:195], v[80:83], v[212:215]\n"
        "ds_read_b64_tr_b16 v[184:185], v240 offset:0\n"
        "v_mfma_f32_16x16x32_bf16 v[216:219], v[196:199], v[80:83], v[216:219]\n"
        "ds_read_b64_tr_b16 v[186:187], v240 offset:512\n"
        "v_mfma_f32_16x16x32_bf16 v[220:223], v[192:195], v[152:155], v[220:223]\n"
        "ds_read_b64_tr_b16 v[188:189], v241 offset:0\n"
        "v_mfma_f32_16x16x32_bf16 v[224:227], v[196:199], v[152:155], v[224:227]\n"
        "ds_read_b64_tr_b16 v[190:191], v241 offset:512\n"
        "s_waitcnt lgkmcnt(8)\n"
        "v_mfma_f32_16x16x32_bf16 v[212:215], v[252:255], v[84:87], v[212:215]\n"
        "ds_write_b64 v30, v[6:7] offset:0\n"
        "ds_write_b64 v31, v[8:9] offset:0\n"
        "v_mfma_f32_16x16x32_bf16 v[216:219], v[2:5], v[84:87], v[216:219]\n"
        "ds_read_b64_tr_b16 v[192:193], v238 offset:61440\n"
        "v_mfma_f32_16x16x32_bf16 v[220:223], v[252:255], v[156:159], v[220:223]\n"
        "ds_read_b64_tr_b16 v[194:195], v238 offset:61952\n"
        "ds_read_b64_tr_b16 v[196:197], v239 offset:61440\n"
        "v_mfma_f32_16x16x32_bf16 v[224:227], v[2:5], v[156:159], v[224:227]\n"
        "ds_read_b64_tr_b16 v[198:199], v239 offset:61952\n"
        "s_waitcnt lgkmcnt(10)\n"
        "v_mfma_f32_16x16x32_bf16 v[212:215], v[176:179], v[88:91], v[212:215]\n"
        "ds_write_b64 v30, v[10:11] offset:4096\n"
        "ds_write_b64 v31, v[12:13] offset:4096\n"
        "v_mfma_f32_16x16x32_bf16 v[216:219], v[180:183], v[88:91], v[216:219]\n"
        "ds_read_b64_tr_b16 v[252:253], v238 offset:57344\n"
        "v_mfma_f32_16x16x32_bf16 v[220:223], v[176:179], v[160:163], v[220:223]\n"
        "ds_read_b64_tr_b16 v[254:255], v238 offset:57856\n"
        "ds_read_b64_tr_b16 v[2:3], v239 offset:57344\n"
        "v_mfma_f32_16x16x32_bf16 v[224:227], v[180:183], v[160:163], v[224:227]\n"
        "ds_read_b64_tr_b16 v[4:5], v239 offset:57856\n"
        "s_waitcnt lgkmcnt(12)\n"
        "v_mfma_f32_16x16x32_bf16 v[212:215], v[184:187], v[92:95], v[212:215]\n"
        "ds_write_b64 v30, v[14:15] offset:8192\n"
        "ds_write_b64 v31, v[16:17] offset:8192\n"
        "v_mfma_f32_16x16x32_bf16 v[216:219], v[188:191], v[92:95], v[216:219]\n"
        "ds_read_b64_tr_b16 v[176:177], v236 offset:61440\n"
        "v_mfma_f32_16x16x32_bf16 v[220:223], v[184:187], v[164:167], v[220:223]\n"
        "ds_read_b64_tr_b16 v[178:179], v236 offset:61952\n"
        "ds_read_b64_tr_b16 v[180:181], v237 offset:61440\n"
        "v_mfma_f32_16x16x32_bf16 v[224:227], v[188:191], v[164:167], v[224:227]\n"
        "ds_read_b64_tr_b16 v[182:183], v237 offset:61952\n"
        "s_waitcnt lgkmcnt(12)\n"
        "v_mfma_f32_16x16x32_bf16 v[212:215], v[192:195], v[96:99], v[212:215]\n"
        "ds_write_b64 v30, v[18:19] offset:12288\n"
        "ds_write_b64 v31, v[20:21] offset:12288\n"
        "v_mfma_f32_16x16x32_bf16 v[216:219], v[196:199], v[96:99], v[216:219]\n"
        "ds_read_b64_tr_b16 v[184:185], v236 offset:36864\n"
        "v_mfma_f32_16x16x32_bf16 v[220:223], v[192:195], v[168:171], v[220:223]\n"
        "ds_read_b64_tr_b16 v[186:187], v236 offset:37376\n"
        "ds_read_b64_tr_b16 v[188:189], v237 offset:36864\n"
        "v_mfma_f32_16x16x32_bf16 v[224:227], v[196:199], v[168:171], v[224:227]\n"
        "ds_read_b64_tr_b16 v[190:191], v237 offset:37376\n"
        "s_waitcnt lgkmcnt(12)\n"
        "v_mfma_f32_16x16x32_bf16 v[212:215], v[252:255], v[100:103], v[212:215]\n"
        "ds_read_b64_tr_b16 v[192:193], v236 offset:32768\n"
        "v_mfma_f32_16x16x32_bf16 v[216:219], v[2:5], v[100:103], v[216:219]\n"
        "ds_read_b64_tr_b16 v[194:195], v236 offset:33280\n"
        "v_mfma_f32_16x16x32_bf16 v[220:223], v[252:255], v[172:175], v[220:223]\n"
        "ds_read_b64_tr_b16 v[196:197], v237 offset:32768\n"
        "v_mfma_f32_16x16x32_bf16 v[224:227], v[2:5], v[172:175], v[224:227]\n"
        "ds_read_b64_tr_b16 v[198:199], v237 offset:33280\n"
        "s_waitcnt lgkmcnt(10)\n"
        "v_cvt_pk_bf16_f32 v200, v212, v213\n"
        "v_cvt_pk_bf16_f32 v201, v214, v215\n"
        "v_mfma_f32_16x16x32_bf16 v[212:215], v[176:179], v[32:35], 0\n"
        "ds_read_b64_tr_b16 v[252:253], v236 offset:28672\n"
        "ds_read_b64_tr_b16 v[254:255], v236 offset:29184\n"
        "v_cvt_pk_bf16_f32 v202, v216, v217\n"
        "v_cvt_pk_bf16_f32 v203, v218, v219\n"
        "v_mfma_f32_16x16x32_bf16 v[216:219], v[180:183], v[32:35], 0\n"
        "ds_read_b64_tr_b16 v[2:3], v237 offset:28672\n"
        "ds_read_b64_tr_b16 v[4:5], v237 offset:29184\n"
        "v_cvt_pk_bf16_f32 v204, v220, v221\n"
        "v_cvt_pk_bf16_f32 v205, v222, v223\n"
        "v_cvt_pk_bf16_f32 v206, v224, v225\n"
        "v_cvt_pk_bf16_f32 v207, v226, v227\n"
        "s_waitcnt lgkmcnt(8)\n"
        "s_barrier\n"
        "v_mfma_f32_16x16x32_bf16 v[212:215], v[184:187], v[36:39], v[212:215]\n"
        "ds_write_b64 v30, v[22:23] offset:16384\n"
        "ds_write_b64 v31, v[24:25] offset:16384\n"
        "ds_read_b64_tr_b16 v[176:177], v240 offset:28672\n"
        "v_mfma_f32_16x16x32_bf16 v[216:219], v[188:191], v[36:39], v[216:219]\n"
        "ds_read_b64_tr_b16 v[178:179], v240 offset:29184\n"
        "ds_read_b64_tr_b16 v[180:181], v241 offset:28672\n"
        "ds_read_b64_tr_b16 v[182:183], v241 offset:29184\n"
        "s_waitcnt lgkmcnt(10)\n"
        "v_mfma_f32_16x16x32_bf16 v[212:215], v[192:195], v[40:43], v[212:215]\n"
        "ds_write_b64 v30, v[26:27] offset:20480\n"
        "ds_write_b64 v31, v[28:29] offset:20480\n"
        "ds_read_b64_tr_b16 v[184:185], v240 offset:24576\n"
        "v_mfma_f32_16x16x32_bf16 v[216:219], v[196:199], v[40:43], v[216:219]\n"
        "ds_read_b64_tr_b16 v[186:187], v240 offset:25088\n"
        "ds_read_b64_tr_b16 v[188:189], v241 offset:24576\n"
        "ds_read_b64_tr_b16 v[190:191], v241 offset:25088\n"
        "s_waitcnt lgkmcnt(12)\n"
        "v_mfma_f32_16x16x32_bf16 v[212:215], v[252:255], v[44:47], v[212:215]\n"
        "ds_write_b64 v30, v[200:201] offset:36864\n"
        "ds_write_b64 v31, v[202:203] offset:36864\n"
        "ds_read_b64_tr_b16 v[192:193], v240 offset:20480\n"
        "v_mfma_f32_16x16x32_bf16 v[216:219], v[2:5], v[44:47], v[216:219]\n"
        "ds_read_b64_tr_b16 v[194:195], v240 offset:20992\n"
        "ds_read_b64_tr_b16 v[196:197], v241 offset:20480\n"
        "ds_read_b64_tr_b16 v[198:199], v241 offset:20992\n"
        "s_waitcnt lgkmcnt(12)\n"
        "v_mfma_f32_16x16x32_bf16 v[212:215], v[176:179], v[48:51], v[212:215]\n"
        "ds_write_b64 v30, v[204:205] offset:40960\n"
        "ds_write_b64 v31, v[206:207] offset:40960\n"
        "ds_read_b64_tr_b16 v[252:253], v240 offset:16384\n"
        "v_mfma_f32_16x16x32_bf16 v[216:219], v[180:183], v[48:51], v[216:219]\n"
        "ds_read_b64_tr_b16 v[254:255], v240 offset:16896\n"
        "ds_read_b64_tr_b16 v[2:3], v241 offset:16384\n"
        "ds_read_b64_tr_b16 v[4:5], v241 offset:16896\n"
        "s_waitcnt lgkmcnt(12)\n"
        "v_mfma_f32_16x16x32_bf16 v[212:215], v[184:187], v[52:55], v[212:215]\n"
        "ds_read_b64_tr_b16 v[176:177], v238 offset:32768\n"
        "ds_read_b64_tr_b16 v[178:179], v238 offset:33280\n"
        "v_mfma_f32_16x16x32_bf16 v[216:219], v[188:191], v[52:55], v[216:219]\n"
        "ds_read_b64_tr_b16 v[180:181], v239 offset:32768\n"
        "ds_read_b64_tr_b16 v[182:183], v239 offset:33280\n"
        "s_waitcnt lgkmcnt(10)\n"
        "v_mfma_f32_16x16x32_bf16 v[212:215], v[192:195], v[56:59], v[212:215]\n"
        "ds_read_b64_tr_b16 v[184:185], v238 offset:28672\n"
        "ds_read_b64_tr_b16 v[186:187], v238 offset:29184\n"
        "v_mfma_f32_16x16x32_bf16 v[216:219], v[196:199], v[56:59], v[216:219]\n"
        "ds_read_b64_tr_b16 v[188:189], v239 offset:28672\n"
        "ds_read_b64_tr_b16 v[190:191], v239 offset:29184\n"
        "s_waitcnt lgkmcnt(8)\n"
        "v_mfma_f32_16x16x32_bf16 v[212:215], v[252:255], v[60:63], v[212:215]\n"
        "ds_read_b64_tr_b16 v[192:193], v236 offset:57344\n"
        "ds_read_b64_tr_b16 v[194:195], v236 offset:57856\n"
        "v_mfma_f32_16x16x32_bf16 v[216:219], v[2:5], v[60:63], v[216:219]\n"
        "ds_read_b64_tr_b16 v[196:197], v237 offset:57344\n"
        "ds_read_b64_tr_b16 v[198:199], v237 offset:57856\n"
        "s_waitcnt lgkmcnt(8)\n"
        "s_barrier\n"
        "v_mfma_f32_16x16x32_bf16 v[212:215], v[176:179], v[64:67], v[212:215]\n"
        "ds_read_b64_tr_b16 v[252:253], v236 offset:53248\n"
        "ds_read_b64_tr_b16 v[254:255], v236 offset:53760\n"
        "v_mfma_f32_16x16x32_bf16 v[216:219], v[180:183], v[64:67], v[216:219]\n"
        "ds_read_b64_tr_b16 v[2:3], v237 offset:53248\n"
        "ds_read_b64_tr_b16 v[4:5], v237 offset:53760\n"
        "s_waitcnt lgkmcnt(8)\n"
        "v_mfma_f32_16x16x32_bf16 v[212:215], v[184:187], v[68:71], v[212:215]\n"
        "ds_read_b64_tr_b16 v[176:177], v236 offset:49152\n"
        "ds_read_b64_tr_b16 v[178:179], v236 offset:49664\n"
        "v_mfma_f32_16x16x32_bf16 v[216:219], v[188:191], v[68:71], v[216:219]\n"
        "ds_read_b64_tr_b16 v[180:181], v237 offset:49152\n"
        "ds_read_b64_tr_b16 v[182:183], v237 offset:49664\n"
        "s_waitcnt lgkmcnt(8)\n"
        "v_mfma_f32_16x16x32_bf16 v[212:215], v[192:195], v[72:75], v[212:215]\n"
        "ds_read_b64_tr_b16 v[184:185], v236 offset:24576\n"
        "ds_read_b64_tr_b16 v[186:187], v236 offset:25088\n"
        "v_mfma_f32_16x16x32_bf16 v[216:219], v[196:199], v[72:75], v[216:219]\n"
        "ds_read_b64_tr_b16 v[188:189], v237 offset:24576\n"
        "ds_read_b64_tr_b16 v[190:191], v237 offset:25088\n"
        "s_waitcnt lgkmcnt(8)\n"
        "v_mfma_f32_16x16x32_bf16 v[212:215], v[252:255], v[76:79], v[212:215]\n"
        "ds_read_b64_tr_b16 v[192:193], v238 offset:53248\n"
        "ds_read_b64_tr_b16 v[194:195], v238 offset:53760\n"
        "v_mfma_f32_16x16x32_bf16 v[216:219], v[2:5], v[76:79], v[216:219]\n"
        "ds_read_b64_tr_b16 v[196:197], v239 offset:53248\n"
        "ds_read_b64_tr_b16 v[198:199], v239 offset:53760\n"
        "s_waitcnt lgkmcnt(8)\n"
        "v_mfma_f32_16x16x32_bf16 v[212:215], v[176:179], v[80:83], v[212:215]\n"
        "ds_read_b64_tr_b16 v[252:253], v238 offset:49152\n"
        "ds_read_b64_tr_b16 v[254:255], v238 offset:49664\n"
        "v_mfma_f32_16x16x32_bf16 v[216:219], v[180:183], v[80:83], v[216:219]\n"
        "ds_read_b64_tr_b16 v[2:3], v239 offset:49152\n"
        "ds_read_b64_tr_b16 v[4:5], v239 offset:49664\n"
        "s_waitcnt lgkmcnt(8)\n"
        "v_mfma_f32_16x16x32_bf16 v[212:215], v[184:187], v[84:87], v[212:215]\n"
        "ds_read_b64_tr_b16 v[176:177], v238 offset:45056\n"
        "ds_read_b64_tr_b16 v[178:179], v238 offset:45568\n"
        "v_mfma_f32_16x16x32_bf16 v[216:219], v[188:191], v[84:87], v[216:219]\n"
        "ds_read_b64_tr_b16 v[180:181], v239 offset:45056\n"
        "ds_read_b64_tr_b16 v[182:183], v239 offset:45568\n"
        "s_waitcnt lgkmcnt(8)\n"
        "v_mfma_f32_16x16x32_bf16 v[212:215], v[192:195], v[88:91], v[212:215]\n"
        "ds_read_b64_tr_b16 v[184:185], v238 offset:40960\n"
        "ds_read_b64_tr_b16 v[186:187], v238 offset:41472\n"
        "v_mfma_f32_16x16x32_bf16 v[216:219], v[196:199], v[88:91], v[216:219]\n"
        "ds_read_b64_tr_b16 v[188:189], v239 offset:40960\n"
        "ds_read_b64_tr_b16 v[190:191], v239 offset:41472\n"
        "s_waitcnt lgkmcnt(8)\n"
        "v_mfma_f32_16x16x32_bf16 v[212:215], v[252:255], v[92:95], v[212:215]\n"
        "v_mfma_f32_16x16x32_bf16 v[216:219], v[2:5], v[92:95], v[216:219]\n"
        "s_waitcnt lgkmcnt(4)\n"
        "v_mfma_f32_16x16x32_bf16 v[212:215], v[176:179], v[96:99], v[212:215]\n"
        "v_mfma_f32_16x16x32_bf16 v[216:219], v[180:183], v[96:99], v[216:219]\n"
        "s_waitcnt lgkmcnt(0)\n"
        "v_mfma_f32_16x16x32_bf16 v[212:215], v[184:187], v[100:103], v[212:215]\n"
        "v_mfma_f32_16x16x32_bf16 v[216:219], v[188:191], v[100:103], v[216:219]\n"
        "s_nop 6\n"
        "v_cvt_pk_bf16_f32 v6, v212, v213\n"
        "v_cvt_pk_bf16_f32 v7, v214, v215\n"
        "ds_write_b64 v30, v[6:7] offset:0\n"
        "v_cvt_pk_bf16_f32 v8, v216, v217\n"
        "v_cvt_pk_bf16_f32 v9, v218, v219\n"
        "ds_write_b64 v31, v[8:9] offset:0\n"
        "s_waitcnt lgkmcnt(0)\n"
        "s_barrier\n"
        "s_branch L%=_end\n"
        "L%=_bhelper:\n"
        "s_waitcnt vmcnt(6) lgkmcnt(0)\n"
        "s_barrier\n"
        "s_add_u32 m0, s46, 159744\n"
        "s_add_u32 s51, s37, 0\n"
        "buffer_load_dwordx4 v14, s[20:23], s51 offen lds\n"
        "s_add_u32 m0, s46, 155648\n"
        "s_add_u32 s51, s37, 36864\n"
        "buffer_load_dwordx4 v14, s[20:23], s51 offen lds\n"
        "s_add_u32 m0, s46, 77824\n"
        "s_add_u32 s51, s37, 73728\n"
        "buffer_load_dwordx4 v14, s[20:23], s51 offen lds\n"
        "s_add_u32 m0, s46, 73728\n"
        "s_add_u32 s51, s37, 110592\n"
        "buffer_load_dwordx4 v14, s[20:23], s51 offen lds\n"
        "v_bfe_u32 v3, v0, 3, 5\n"
        "v_and_b32 v89, 7, v0\n"
        "v_mul_u32_u24 v250, 1152, v3\n"
        "v_lshl_add_u32 v250, v89, 4, v250\n"
        "v_and_b32 v4, 7, v3\n"
        "v_xor_b32 v4, v4, v89\n"
        "v_lshlrev_b32 v4, 4, v4\n"
        "v_lshl_add_u32 v251, v3, 7, v4\n"
        "s_mov_b32 s26, 9437184\n"
        "s_mov_b32 s27, 0x20000\n"
        "s_waitcnt vmcnt(7)\n"
        "s_barrier\n"
        "s_add_u32 m0, s46, 98304\n"
        "s_add_u32 s51, s37, 147456\n"
        "buffer_load_dwordx4 v14, s[20:23], s51 offen lds\n"
        "s_add_u32 m0, s46, 94208\n"
        "s_add_u32 s51, s37, 184320\n"
        "buffer_load_dwordx4 v14, s[20:23], s51 offen lds\n"
        "s_add_u32 m0, s46, 90112\n"
        "s_add_u32 s51, s37, 221184\n"
        "buffer_load_dwordx4 v14, s[20:23], s51 offen lds\n"
        "s_add_u32 m0, s46, 86016\n"
        "s_add_u32 s51, s37, 258048\n"
        "buffer_load_dwordx4 v14, s[20:23], s51 offen lds\n"
        "s_add_u32 m0, s46, 81920\n"
        "s_add_u32 s51, s37, 294912\n"
        "buffer_load_dwordx4 v14, s[20:23], s51 offen lds\n"
        "s_waitcnt vmcnt(9)\n"
        "s_barrier\n"
        "s_add_u32 m0, s46, 118784\n"
        "s_add_u32 s51, s37, 331776\n"
        "buffer_load_dwordx4 v14, s[20:23], s51 offen lds\n"
        "s_add_u32 m0, s46, 114688\n"
        "s_add_u32 s51, s37, 368640\n"
        "buffer_load_dwordx4 v14, s[20:23], s51 offen lds\n"
        "s_add_u32 m0, s46, 110592\n"
        "s_add_u32 s51, s37, 405504\n"
        "buffer_load_dwordx4 v14, s[20:23], s51 offen lds\n"
        "s_add_u32 m0, s46, 106496\n"
        "s_add_u32 s51, s37, 442368\n"
        "buffer_load_dwordx4 v14, s[20:23], s51 offen lds\n"
        "s_add_u32 m0, s46, 102400\n"
        "s_add_u32 s51, s37, 479232\n"
        "buffer_load_dwordx4 v14, s[20:23], s51 offen lds\n"
        "s_add_u32 m0, s46, 36864\n"
        "s_add_u32 s51, s37, 516096\n"
        "buffer_load_dwordx4 v14, s[20:23], s51 offen lds\n"
        "s_add_u32 m0, s46, 32768\n"
        "s_add_u32 s51, s37, 552960\n"
        "buffer_load_dwordx4 v14, s[20:23], s51 offen lds\n"
        "s_add_u32 m0, s46, 28672\n"
        "s_add_u32 s51, s37, 589824\n"
        "buffer_load_dwordx4 v14, s[20:23], s51 offen lds\n"
        "s_add_u32 m0, s46, 24576\n"
        "s_add_u32 s51, s37, 626688\n"
        "buffer_load_dwordx4 v14, s[20:23], s51 offen lds\n"
        "s_waitcnt vmcnt(9)\n"
        "s_barrier\n"
        "s_add_u32 m0, s46, 143360\n"
        "s_add_u32 s51, s38, 0\n"
        "buffer_load_dwordx4 v14, s[20:23], s51 offen lds\n"
        "s_add_u32 m0, s46, 139264\n"
        "s_add_u32 s51, s38, 36864\n"
        "buffer_load_dwordx4 v14, s[20:23], s51 offen lds\n"
        "s_add_u32 m0, s46, 135168\n"
        "s_add_u32 s51, s38, 73728\n"
        "buffer_load_dwordx4 v14, s[20:23], s51 offen lds\n"
        "s_add_u32 m0, s46, 131072\n"
        "s_add_u32 s51, s38, 110592\n"
        "buffer_load_dwordx4 v14, s[20:23], s51 offen lds\n"
        "s_add_u32 m0, s46, 126976\n"
        "s_add_u32 s51, s38, 147456\n"
        "buffer_load_dwordx4 v14, s[20:23], s51 offen lds\n"
        "s_add_u32 m0, s46, 122880\n"
        "s_add_u32 s51, s38, 184320\n"
        "buffer_load_dwordx4 v14, s[20:23], s51 offen lds\n"
        "s_add_u32 m0, s46, 61440\n"
        "s_add_u32 s51, s38, 221184\n"
        "buffer_load_dwordx4 v14, s[20:23], s51 offen lds\n"
        "s_add_u32 m0, s46, 57344\n"
        "s_add_u32 s51, s38, 258048\n"
        "buffer_load_dwordx4 v14, s[20:23], s51 offen lds\n"
        "s_add_u32 m0, s46, 53248\n"
        "s_add_u32 s51, s38, 294912\n"
        "buffer_load_dwordx4 v14, s[20:23], s51 offen lds\n"
        "s_add_u32 m0, s46, 49152\n"
        "s_add_u32 s51, s38, 331776\n"
        "buffer_load_dwordx4 v14, s[20:23], s51 offen lds\n"
        "s_waitcnt vmcnt(10)\n"
        "s_barrier\n"
        "s_add_u32 m0, s46, 159744\n"
        "s_add_u32 s51, s38, 368640\n"
        "buffer_load_dwordx4 v14, s[20:23], s51 offen lds\n"
        "s_add_u32 m0, s46, 155648\n"
        "s_add_u32 s51, s38, 405504\n"
        "buffer_load_dwordx4 v14, s[20:23], s51 offen lds\n"
        "s_add_u32 m0, s46, 151552\n"
        "s_add_u32 s51, s38, 442368\n"
        "buffer_load_dwordx4 v14, s[20:23], s51 offen lds\n"
        "s_add_u32 m0, s46, 147456\n"
        "s_add_u32 s51, s38, 479232\n"
        "buffer_load_dwordx4 v14, s[20:23], s51 offen lds\n"
        "s_add_u32 m0, s46, 98304\n"
        "s_add_u32 s51, s38, 516096\n"
        "buffer_load_dwordx4 v14, s[20:23], s51 offen lds\n"
        "s_add_u32 m0, s46, 94208\n"
        "s_add_u32 s51, s38, 552960\n"
        "buffer_load_dwordx4 v14, s[20:23], s51 offen lds\n"
        "s_add_u32 m0, s46, 90112\n"
        "s_add_u32 s51, s38, 589824\n"
        "buffer_load_dwordx4 v14, s[20:23], s51 offen lds\n"
        "s_add_u32 m0, s46, 77824\n"
        "s_add_u32 s51, s38, 626688\n"
        "buffer_load_dwordx4 v14, s[20:23], s51 offen lds\n"
        "s_add_u32 m0, s46, 73728\n"
        "s_add_u32 s51, s39, 0\n"
        "buffer_load_dwordx4 v14, s[20:23], s51 offen lds\n"
        "s_add_u32 m0, s46, 69632\n"
        "s_add_u32 s51, s39, 36864\n"
        "buffer_load_dwordx4 v14, s[20:23], s51 offen lds\n"
        "s_waitcnt vmcnt(11)\n"
        "s_barrier\n"
        "s_add_u32 m0, s46, 118784\n"
        "s_add_u32 s51, s39, 73728\n"
        "buffer_load_dwordx4 v14, s[20:23], s51 offen lds\n"
        "s_add_u32 m0, s46, 114688\n"
        "s_add_u32 s51, s39, 110592\n"
        "buffer_load_dwordx4 v14, s[20:23], s51 offen lds\n"
        "s_add_u32 m0, s46, 110592\n"
        "s_add_u32 s51, s39, 147456\n"
        "buffer_load_dwordx4 v14, s[20:23], s51 offen lds\n"
        "s_add_u32 m0, s46, 106496\n"
        "s_add_u32 s51, s39, 184320\n"
        "buffer_load_dwordx4 v14, s[20:23], s51 offen lds\n"
        "s_add_u32 m0, s46, 102400\n"
        "s_add_u32 s51, s39, 221184\n"
        "buffer_load_dwordx4 v14, s[20:23], s51 offen lds\n"
        "s_add_u32 m0, s46, 86016\n"
        "s_add_u32 s51, s39, 258048\n"
        "buffer_load_dwordx4 v14, s[20:23], s51 offen lds\n"
        "s_add_u32 m0, s46, 81920\n"
        "s_add_u32 s51, s39, 294912\n"
        "buffer_load_dwordx4 v14, s[20:23], s51 offen lds\n"
        "s_add_u32 m0, s46, 65536\n"
        "s_add_u32 s51, s39, 331776\n"
        "buffer_load_dwordx4 v14, s[20:23], s51 offen lds\n"
        "s_add_u32 m0, s46, 45056\n"
        "s_add_u32 s51, s39, 368640\n"
        "buffer_load_dwordx4 v14, s[20:23], s51 offen lds\n"
        "s_add_u32 m0, s46, 40960\n"
        "s_add_u32 s51, s39, 405504\n"
        "buffer_load_dwordx4 v14, s[20:23], s51 offen lds\n"
        "s_waitcnt vmcnt(12)\n"
        "s_barrier\n"
        "s_add_u32 m0, s46, 143360\n"
        "s_add_u32 s51, s39, 442368\n"
        "buffer_load_dwordx4 v14, s[20:23], s51 offen lds\n"
        "s_add_u32 m0, s46, 139264\n"
        "s_add_u32 s51, s39, 479232\n"
        "buffer_load_dwordx4 v14, s[20:23], s51 offen lds\n"
        "s_add_u32 m0, s46, 135168\n"
        "s_add_u32 s51, s39, 516096\n"
        "buffer_load_dwordx4 v14, s[20:23], s51 offen lds\n"
        "s_add_u32 m0, s46, 131072\n"
        "s_add_u32 s51, s39, 552960\n"
        "buffer_load_dwordx4 v14, s[20:23], s51 offen lds\n"
        "s_add_u32 m0, s46, 126976\n"
        "s_add_u32 s51, s39, 589824\n"
        "buffer_load_dwordx4 v14, s[20:23], s51 offen lds\n"
        "s_add_u32 m0, s46, 122880\n"
        "s_add_u32 s51, s39, 626688\n"
        "buffer_load_dwordx4 v14, s[20:23], s51 offen lds\n"
        "s_add_u32 m0, s46, 61440\n"
        "s_add_u32 s51, s40, 0\n"
        "buffer_load_dwordx4 v14, s[20:23], s51 offen lds\n"
        "s_add_u32 m0, s46, 36864\n"
        "s_add_u32 s51, s40, 36864\n"
        "buffer_load_dwordx4 v14, s[20:23], s51 offen lds\n"
        "s_add_u32 m0, s46, 32768\n"
        "s_add_u32 s51, s40, 73728\n"
        "buffer_load_dwordx4 v14, s[20:23], s51 offen lds\n"
        "s_add_u32 m0, s46, 28672\n"
        "s_add_u32 s51, s40, 110592\n"
        "buffer_load_dwordx4 v14, s[20:23], s51 offen lds\n"
        "s_waitcnt vmcnt(13)\n"
        "s_barrier\n"
        "s_add_u32 m0, s46, 159744\n"
        "s_add_u32 s51, s40, 147456\n"
        "buffer_load_dwordx4 v14, s[20:23], s51 offen lds\n"
        "s_add_u32 m0, s46, 155648\n"
        "s_add_u32 s51, s40, 184320\n"
        "buffer_load_dwordx4 v14, s[20:23], s51 offen lds\n"
        "s_add_u32 m0, s46, 151552\n"
        "s_add_u32 s51, s40, 221184\n"
        "buffer_load_dwordx4 v14, s[20:23], s51 offen lds\n"
        "s_add_u32 m0, s46, 147456\n"
        "s_add_u32 s51, s40, 258048\n"
        "buffer_load_dwordx4 v14, s[20:23], s51 offen lds\n"
        "s_add_u32 m0, s46, 98304\n"
        "s_add_u32 s51, s40, 294912\n"
        "buffer_load_dwordx4 v14, s[20:23], s51 offen lds\n"
        "s_add_u32 m0, s46, 94208\n"
        "s_add_u32 s51, s40, 331776\n"
        "buffer_load_dwordx4 v14, s[20:23], s51 offen lds\n"
        "s_add_u32 m0, s46, 57344\n"
        "s_add_u32 s51, s40, 368640\n"
        "buffer_load_dwordx4 v14, s[20:23], s51 offen lds\n"
        "s_add_u32 m0, s46, 53248\n"
        "s_add_u32 s51, s40, 405504\n"
        "buffer_load_dwordx4 v14, s[20:23], s51 offen lds\n"
        "s_add_u32 m0, s46, 49152\n"
        "s_add_u32 s51, s40, 442368\n"
        "buffer_load_dwordx4 v14, s[20:23], s51 offen lds\n"
        "s_add_u32 m0, s46, 24576\n"
        "s_add_u32 s51, s40, 479232\n"
        "buffer_load_dwordx4 v14, s[20:23], s51 offen lds\n"
        "s_waitcnt vmcnt(10)\n"
        "s_barrier\n"
        "s_add_u32 m0, s46, 118784\n"
        "s_add_u32 s51, s40, 516096\n"
        "buffer_load_dwordx4 v14, s[20:23], s51 offen lds\n"
        "s_add_u32 m0, s46, 114688\n"
        "s_add_u32 s51, s40, 552960\n"
        "buffer_load_dwordx4 v14, s[20:23], s51 offen lds\n"
        "s_add_u32 m0, s46, 110592\n"
        "s_add_u32 s51, s40, 589824\n"
        "buffer_load_dwordx4 v14, s[20:23], s51 offen lds\n"
        "s_add_u32 m0, s46, 106496\n"
        "s_add_u32 s51, s40, 626688\n"
        "buffer_load_dwordx4 v14, s[20:23], s51 offen lds\n"
        "s_waitcnt vmcnt(7)\n"
        "s_barrier\n"
        "ds_read_b128 v[40:43], v251 offset:0\n"
        "ds_read_b128 v[44:47], v251 offset:4096\n"
        "s_add_u32 s51, s33, s36\n"
        "s_waitcnt lgkmcnt(1)\n"
        "buffer_store_dwordx4 v[40:43], v250, s[24:27], s51 offen sc1 nt\n"
        "s_add_u32 s51, s34, s36\n"
        "s_waitcnt lgkmcnt(0)\n"
        "buffer_store_dwordx4 v[44:47], v250, s[24:27], s51 offen sc1 nt\n"
        "ds_read_b128 v[56:59], v251 offset:8192\n"
        "ds_read_b128 v[60:63], v251 offset:12288\n"
        "s_add_u32 s51, s33, s37\n"
        "s_waitcnt lgkmcnt(1)\n"
        "buffer_store_dwordx4 v[56:59], v250, s[24:27], s51 offen sc1 nt\n"
        "s_add_u32 s51, s34, s37\n"
        "s_waitcnt lgkmcnt(0)\n"
        "buffer_store_dwordx4 v[60:63], v250, s[24:27], s51 offen sc1 nt\n"
        "s_waitcnt vmcnt(4)\n"
        "s_barrier\n"
        "ds_read_b128 v[72:75], v251 offset:16384\n"
        "ds_read_b128 v[76:79], v251 offset:20480\n"
        "s_add_u32 s51, s33, s38\n"
        "s_waitcnt lgkmcnt(1)\n"
        "buffer_store_dwordx4 v[72:75], v250, s[24:27], s51 offen sc1 nt\n"
        "s_add_u32 s51, s34, s38\n"
        "s_waitcnt lgkmcnt(0)\n"
        "buffer_store_dwordx4 v[76:79], v250, s[24:27], s51 offen sc1 nt\n"
        "ds_read_b128 v[88:91], v251 offset:36864\n"
        "ds_read_b128 v[92:95], v251 offset:40960\n"
        "s_add_u32 s51, s33, s39\n"
        "s_waitcnt lgkmcnt(1)\n"
        "buffer_store_dwordx4 v[88:91], v250, s[24:27], s51 offen sc1 nt\n"
        "s_add_u32 s51, s34, s39\n"
        "s_waitcnt lgkmcnt(0)\n"
        "buffer_store_dwordx4 v[92:95], v250, s[24:27], s51 offen sc1 nt\n"
        "s_barrier\n"
        "ds_read_b128 v[104:107], v251 offset:0\n"
        "s_add_u32 s51, s33, s40\n"
        "s_waitcnt lgkmcnt(0)\n"
        "buffer_store_dwordx4 v[104:107], v250, s[24:27], s51 offen sc1 nt\n"
        "s_branch L%=_end\n"
        "L%=_end:\n"
        :
        : "s"(__builtin_amdgcn_kernarg_segment_ptr()), "s"(blockIdx.x), "v"(threadIdx.x),
          "s"((unsigned)(uintptr_t)(__attribute__((address_space(3))) char*)lds)
        : "v1", "v2", "v3", "v4", "v5", "v6", "v7", "v8", "v9", "v10", "v11", "v12", "v13", "v14", "v15", "v16", "v17", "v18", "v19", "v20", "v21", "v22", "v23", "v24", "v25", "v26", "v27", "v28", "v29", "v30", "v31", "v32", "v33", "v34", "v35", "v36", "v37", "v38", "v39", "v40", "v41", "v42", "v43", "v44", "v45", "v46", "v47", "v48", "v49", "v50", "v51", "v52", "v53", "v54", "v55", "v56", "v57", "v58", "v59", "v60", "v61", "v62", "v63", "v64", "v65", "v66", "v67", "v68", "v69", "v70", "v71", "v72", "v73", "v74", "v75", "v76", "v77", "v78", "v79", "v80", "v81", "v82", "v83", "v84", "v85", "v86", "v87", "v88", "v89", "v90", "v91", "v92", "v93", "v94", "v95", "v96", "v97", "v98", "v99", "v100", "v101", "v102", "v103", "v104", "v105", "v106", "v107", "v108", "v109", "v110", "v111", "v112", "v113", "v114", "v115", "v116", "v117", "v118", "v119", "v120", "v121", "v122", "v123", "v124", "v125", "v126", "v127", "v128", "v129", "v130", "v131", "v132", "v133", "v134", "v135", "v136", "v137", "v138", "v139", "v140", "v141", "v142", "v143", "v144", "v145", "v146", "v147", "v148", "v149", "v150", "v151", "v152", "v153", "v154", "v155", "v156", "v157", "v158", "v159", "v160", "v161", "v162", "v163", "v164", "v165", "v166", "v167", "v168", "v169", "v170", "v171", "v172", "v173", "v174", "v175", "v176", "v177", "v178", "v179", "v180", "v181", "v182", "v183", "v184", "v185", "v186", "v187", "v188", "v189", "v190", "v191", "v192", "v193", "v194", "v195", "v196", "v197", "v198", "v199", "v200", "v201", "v202", "v203", "v204", "v205", "v206", "v207", "v208", "v209", "v210", "v211", "v212", "v213", "v214", "v215", "v216", "v217", "v218", "v219", "v220", "v221", "v222", "v223", "v224", "v225", "v226", "v227", "v228", "v229", "v230", "v231", "v232", "v233", "v234", "v235", "v236", "v237", "v238", "v239", "v240", "v241", "v242", "v243", "v244", "v245", "v246", "v247", "v248", "v249", "v250", "v251", "v252", "v253", "v254", "v255", "s8", "s9", "s10", "s11", "s12", "s13", "s14", "s15", "s16", "s17", "s18", "s19", "s20", "s21", "s22", "s23", "s24", "s25", "s26", "s27", "s28", "s29", "s30", "s31", "s33", "s34", "s35", "s36", "s37", "s38", "s39", "s40", "s41", "s42", "s43", "s44", "s45", "s46", "s47", "s48", "s49", "s50", "s51", "s52", "s53", "s54", "s55", "s56", "s57", "s58", "s59", "s60", "s61", "s62", "s63", "s64", "s65", "s66", "s67", "s68", "s69", "s70", "s71", "s72", "s73", "s74", "s75", "s76", "s77", "s78", "s79", "s80", "s81", "s82", "s83", "s84", "s85", "s86", "s87", "s88", "s89", "s90", "s91", "s92", "s93", "vcc", "scc", "memory");
}

extern "C" void kernel_launch(void* const* d_in, const int* in_sizes, int n_in, void* d_out, int out_size,
                              void* d_ws, size_t ws_size, hipStream_t stream) {
    ln_linear_kernel<<<256, 512, 0, stream>>>(d_in[0], d_in[1], d_in[2], d_in[3], d_out);
}
```
